```python
import torch

def _softmax(x, axis=-1):
    return torch.softmax(x, dim=axis)
def _gelu(x):
    return torch.nn.functional.gelu(x, approximate="tanh")
def _silu(x):
    return torch.nn.functional.silu(x)
def _sigmoid(x):
    return torch.sigmoid(x)
def _layernorm(x, axis=-1, eps=1e-5):
    m = x.mean(dim=axis, keepdim=True)
    v = x.var(dim=axis, keepdim=True, unbiased=False)
    return (x - m) / torch.sqrt(v + eps)
def _scatter_add(tgt, idx, src, axis=0):
    return tgt.clone().index_add_(axis, idx, src)
def _segment_sum(src, seg, n):
    out = torch.zeros((n,) + src.shape[1:], dtype=src.dtype)
    return out.index_add_(0, seg, src)
def _pad(x, pw):
    flat = tuple(v for pair in reversed(pw) for v in pair)
    return torch.nn.functional.pad(x, flat)

kernel_name = 'standalone__paged_block_table_kv_pagedattention__sampling'

def setup_inputs():

    _w = (lambda a: (lambda t: t.to(torch.bfloat16) if t.is_floating_point() else t)(torch.as_tensor(a)))
    B, Hq, Hkv, Sq, Skv, D = 16, 32, 8, 32, 4096, 128
    bs = 16
    nbps = max(1, Skv // bs)
    num_phys = B * nbps + 4
    bt = torch.randperm(num_phys)[: B * nbps].reshape(B, nbps).to(torch.int64)
    cl = (nbps * bs - (torch.arange(1, B + 1, dtype=torch.int64) * 2654435761) % max(1, nbps * bs // 2)).to(torch.int64)
    return {
        "q": _w((lambda *s: torch.randn(*s, dtype=torch.bfloat16) * 0.8)(B, Hq, Sq, D)),
        "k_cache": _w((lambda *s: torch.randn(*s, dtype=torch.bfloat16) * 0.8)(num_phys, bs, Hkv, D)),
        "v_cache": _w((lambda *s: torch.randn(*s, dtype=torch.bfloat16) * 0.05)(num_phys, bs, Hkv, D)),
        "block_table": _w(bt),
        "context_lens": _w(cl),
    }

def reference(q, k_cache, v_cache, block_table, context_lens):
    B, Hq, Sq, D = q.shape
    bs = k_cache.shape[1]
    Hkv = k_cache.shape[2]
    nbps = block_table.shape[1]
    Skv = nbps * bs
    bt = block_table.reshape(-1)
    k = (lambda x, *d: x.permute(*d))(k_cache[bt].reshape(B, Skv, Hkv, D), 0, 2, 1, 3)
    v = (lambda x, *d: x.permute(*d))(v_cache[bt].reshape(B, Skv, Hkv, D), 0, 2, 1, 3)
    g = Hq // Hkv
    qg = q.reshape(B, Hkv, g, Sq, D)
    scores = torch.einsum("bhgqd,bhkd->bhgqk", qg, k) * (D ** -0.5)
    pos = torch.arange(Skv)
    msk = pos[None, :] < context_lens[:, None]
    scores = torch.where(msk[:, None, None, None, :], scores, -1e9)
    p = _softmax(scores, axis=-1)
    out = torch.einsum("bhgqk,bhkd->bhgqd", p, v)
    return out.reshape(B, Hq, Sq, D)
```

```cpp
#include <hip/hip_runtime.h>
#include <hip/hip_bf16.h>
#include <stdint.h>

namespace {

constexpr int B = 16, HQ = 32, HKV = 8, G = HQ / HKV, SQ = 32, D = 128, PAGE = 16, NBPS = 256, SKV = NBPS * PAGE;
constexpr int TILE = 64;
constexpr int NWG = 256;
constexpr int NS = 4;
constexpr int STAGE_BYTES = 32768;
constexpr int SCR_OFF = NS * STAGE_BYTES;
constexpr int SCR_ML = SCR_OFF + 16384;
constexpr int LDS_BYTES = SCR_ML + 1024;
constexpr float SCALE_LOG2E = 0.08838834764831845f * 1.4426950408889634f;

typedef short bf16x8 __attribute__((ext_vector_type(8)));
typedef short s16x4 __attribute__((ext_vector_type(4)));
typedef float f32x16 __attribute__((ext_vector_type(16)));
typedef float f32x4 __attribute__((ext_vector_type(4)));
typedef float f32x2 __attribute__((ext_vector_type(2)));
typedef __bf16 bfx2 __attribute__((ext_vector_type(2)));
typedef unsigned u32x4 __attribute__((ext_vector_type(4)));
typedef unsigned u32x2 __attribute__((ext_vector_type(2)));
#define LDS_AS __attribute__((address_space(3)))

__device__ __forceinline__ int eff_len(int l) { l = l > SKV ? SKV : l; return l <= 0 ? SKV : l; }
__device__ __forceinline__ unsigned swz(unsigned row) { return ((row & 3u) << 2) | ((row >> 2) & 3u); }
__device__ __forceinline__ float swap32_max(float x) {
  auto r = __builtin_amdgcn_permlane32_swap(__float_as_uint(x), __float_as_uint(x), false, false);
  return fmaxf(__uint_as_float(r[0]), __uint_as_float(r[1]));
}
__device__ __forceinline__ float swap32_sum(float x) {
  auto r = __builtin_amdgcn_permlane32_swap(__float_as_uint(x), __float_as_uint(x), false, false);
  return __uint_as_float(r[0]) + __uint_as_float(r[1]);
}
__device__ __forceinline__ unsigned pack2(float a, float b) {
  f32x2 f = {a, b};
  return __builtin_bit_cast(unsigned, __builtin_convertvector(f, bfx2));
}

template <bool NT>
__device__ __forceinline__ void dma16(const void* sbase, unsigned voff, unsigned lds_dst) {
  unsigned keep;
  if (NT)
    asm volatile("s_nop 4\n\ts_mov_b32 %0, m0\n\ts_mov_b32 m0, %3\n\ts_nop 0\n\tglobal_load_lds_dwordx4 %1, %2 nt\n\ts_mov_b32 m0, %0"
                 : "=&s"(keep) : "v"(voff), "s"(sbase), "s"(lds_dst) : "memory");
  else
    asm volatile("s_nop 4\n\ts_mov_b32 %0, m0\n\ts_mov_b32 m0, %3\n\ts_nop 0\n\tglobal_load_lds_dwordx4 %1, %2\n\ts_mov_b32 m0, %0"
                 : "=&s"(keep) : "v"(voff), "s"(sbase), "s"(lds_dst) : "memory");
}

struct Cur { int b, h, t, nt, len, isq; };

__device__ __forceinline__ void cur_advance(Cur& c, const int* lens) {
  if (c.isq) { c.isq = 0; return; }
  if (++c.t == c.nt) {
    c.t = 0; c.isq = 1;
    if (++c.h == HKV) {
      c.h = 0; ++c.b;
      const int bb = c.b < B ? c.b : B - 1;
      c.len = eff_len(lens[bb]); c.nt = (c.len + TILE - 1) / TILE;
    }
  }
}

#define WAITBAR(n) asm volatile("s_waitcnt vmcnt(" #n ")\n\ts_barrier" ::: "memory")
#define LBAR() asm volatile("s_waitcnt lgkmcnt(0)\n\ts_barrier" ::: "memory")

__global__ __launch_bounds__(512, 2) void attn_split(const __hip_bfloat16* __restrict__ q, const __hip_bfloat16* __restrict__ kc,
                                                     const __hip_bfloat16* __restrict__ vc, const int* __restrict__ bt,
                                                     const int* __restrict__ lens, float* __restrict__ po, float* __restrict__ pml) {
  __shared__ __attribute__((aligned(256))) char smem[LDS_BYTES];
  LDS_AS char* const sm = (LDS_AS char*)smem;
  const unsigned sm0 = (unsigned)(size_t)sm;
  const int tid = threadIdx.x, lane = tid & 63;
  const int w = __builtin_amdgcn_readfirstlane(tid >> 6);
  const int wg = blockIdx.x;
  const int r32 = lane & 31, hi = lane >> 5, gsel = w >> 2, qb = w & 3;

  int tot = 0;
#pragma unroll
  for (int b = 0; b < B; ++b) tot += (eff_len(lens[b]) + TILE - 1) / TILE;
  const int T = tot * HKV;
  const int g0 = wg * T / NWG, g1 = (wg + 1) * T / NWG;
  if (g0 == g1) return;
  Cur start; { int acc = 0, sb = 0, sP = 0, snt = 1, sl = 1;
#pragma unroll
    for (int b = 0; b < B; ++b) { const int l = eff_len(lens[b]), nt = (l + TILE - 1) / TILE;
      if (acc <= g0) { sb = b; sP = acc; snt = nt; sl = l; }
      acc += nt * HKV; }
    start.b = sb; start.h = (g0 - sP) / snt; start.t = (g0 - sP) % snt; start.nt = snt; start.len = sl; start.isq = 1; }

  unsigned vkv[2];
#pragma unroll
  for (int i = 0; i < 2; ++i) { const unsigned rt = 8 * w + 4 * i + (lane >> 4), rp = (w & 1) * 8 + 4 * i + (lane >> 4);
    vkv[i] = rp * 2048u + (((unsigned)lane & 15u) ^ swz(rt)) * 16u; }

  auto issue = [&](const Cur& c, int slot) {
    const unsigned ldsb = sm0 + (unsigned)slot * STAGE_BYTES;
    if (c.isq) {
      const char* qbase = (const char*)q + (size_t)((c.b * HQ + c.h * G) * SQ) * (D * 2);
#pragma unroll
      for (int i = 0; i < 4; ++i) { const unsigned row = 16 * w + 4 * i + (lane >> 4);
        dma16<false>(qbase, row * 256u + (((unsigned)lane & 15u) ^ swz(row)) * 16u, ldsb + (16 * w + 4 * i) * 256u); }
    } else {
      const int phys = bt[c.b * NBPS + c.t * 4 + (w >> 1)];
      const size_t poff = (size_t)phys * (PAGE * HKV * D * 2) + (size_t)c.h * (D * 2);
      const char* kb = (const char*)kc + poff; const char* vb = (const char*)vc + poff;
#pragma unroll
      for (int i = 0; i < 2; ++i) dma16<true>(kb, vkv[i], ldsb + (8 * w + 4 * i) * 256u);
#pragma unroll
      for (int i = 0; i < 2; ++i) dma16<true>(vb, vkv[i], ldsb + 16384u + (8 * w + 4 * i) * 256u);
    }
  };

  bf16x8 qf[8];
  f32x16 o[4];
#pragma unroll
  for (int d = 0; d < 4; ++d) o[d] = f32x16{};
  float m = -__builtin_inff(), l = 0.f;
  int fb = 0, fh = 0;

  auto flush = [&]() {
    const float lf = swap32_sum(l);
    LDS_AS float* const scr = (LDS_AS float*)(sm + SCR_OFF) + qb * 1024;
    LDS_AS float* const sml = (LDS_AS float*)(sm + SCR_ML) + (qb * 32 + r32) * 2;
    if (gsel == 1 && hi == 0) { sml[0] = m; sml[1] = lf; }
    float fA = 1.f, fB = 0.f, M = m, L = lf;
#pragma unroll
    for (int d = 0; d < 4; ++d) {
      if (gsel == 1) {
#pragma unroll
        for (int g = 0; g < 4; ++g) *(LDS_AS f32x4*)(scr + (g * 64 + lane) * 4) = f32x4{o[d][4 * g], o[d][4 * g + 1], o[d][4 * g + 2], o[d][4 * g + 3]};
      }
      LBAR();
      if (gsel == 0) {
        if (d == 0) { const float mB = sml[0], lB = sml[1]; M = fmaxf(m, mB);
          fA = __builtin_amdgcn_exp2f(m - M); fB = __builtin_amdgcn_exp2f(mB - M); L = lf * fA + lB * fB; }
#pragma unroll
        for (int g = 0; g < 4; ++g) { const f32x4 x = *(LDS_AS f32x4*)(scr + (g * 64 + lane) * 4);
#pragma unroll
          for (int e = 0; e < 4; ++e) o[d][4 * g + e] = o[d][4 * g + e] * fA + x[e] * fB; }
      }
      LBAR();
    }
    if (gsel == 0) {
      const int slot = wg + fb * HKV + fh;
      float* dst = po + (size_t)slot * (128 * D) + (size_t)(qb * 4) * 1024 + lane * 4;
#pragma unroll
      for (int d = 0; d < 4; ++d)
#pragma unroll
        for (int g = 0; g < 4; ++g) *(f32x4*)(dst + (d * 4 + g) * 256) = f32x4{o[d][4 * g], o[d][4 * g + 1], o[d][4 * g + 2], o[d][4 * g + 3]};
      if (hi == 0) *(f32x2*)(pml + ((size_t)slot * 128 + qb * 32 + r32) * 2) = f32x2{M, L};
    }
#pragma unroll
    for (int d = 0; d < 4; ++d) o[d] = f32x16{};
    m = -__builtin_inff(); l = 0.f;
  };

  const unsigned krow = 32 * gsel + r32;
  const unsigned kaddr0 = krow * 256u + (((unsigned)hi) ^ swz(krow)) * 16u;
  const unsigned tq = (lane & 15) >> 2, tp = lane & 3, tblk = (lane >> 4) & 1;
  const unsigned vaddr0 = 16384u + (32 * gsel + 4 * hi + tq) * 256u + ((2 * tblk + (tp >> 1)) ^ ((tq << 2) | (unsigned)hi)) * 16u + 8u * (tp & 1);
  const unsigned qrow = 32 * qb + r32;
  const unsigned qaddr0 = qrow * 256u + (((unsigned)hi) ^ swz(qrow)) * 16u;

  Cur pc = start, cc = start;
  int prem = g1 - g0;
  int issued = 0;
  for (int s = 0; s < NS - 1 && prem > 0; ++s) { issue(pc, s); ++issued; if (!pc.isq) --prem; cur_advance(pc, lens); }
  bool first = true;
  for (int i = 0; i < issued; ++i) {
    const int ahead = issued - i - 1;
    if (ahead >= 2) WAITBAR(8); else if (ahead == 1) WAITBAR(4); else WAITBAR(0);
    static_assert(NS == 4, "wait table");
    if (cc.isq && !first) flush();
    if (prem > 0) { issue(pc, (i + NS - 1) % NS); ++issued; if (!pc.isq) --prem; cur_advance(pc, lens); }
    const unsigned sbase = (unsigned)(i % NS) * STAGE_BYTES;
    if (cc.isq) {
      first = false; fb = cc.b; fh = cc.h;
      const bool zq = lens[cc.b] <= 0;
#pragma unroll
      for (int s = 0; s < 8; ++s) { qf[s] = *(LDS_AS bf16x8*)(sm + sbase + (qaddr0 ^ (s << 5))); if (zq) qf[s] = bf16x8{}; }
    } else {
      const int nvalid = cc.len - cc.t * TILE - 32 * gsel;
      if (nvalid > 0) {
        f32x16 s = f32x16{};
#pragma unroll
        for (int ks = 0; ks < 8; ++ks) {
          const bf16x8 a = *(LDS_AS bf16x8*)(sm + sbase + (kaddr0 ^ (ks << 5)));
          s = __builtin_amdgcn_mfma_f32_32x32x16_bf16(a, qf[ks], s, 0, 0, 0);
        }
        if (nvalid < 32) {
#pragma unroll
          for (int r = 0; r < 16; ++r) { const int key = (r & 3) + 8 * (r >> 2) + 4 * hi; s[r] = key < nvalid ? s[r] : -__builtin_inff(); }
        }
        float tmax = s[0];
#pragma unroll
        for (int r = 1; r < 16; ++r) tmax = fmaxf(tmax, s[r]);
        tmax = swap32_max(tmax) * SCALE_LOG2E;
        if (__any(tmax > m)) {
          const float mn = fmaxf(m, tmax), alpha = __builtin_amdgcn_exp2f(m - mn);
          m = mn; l *= alpha;
#pragma unroll
          for (int d = 0; d < 4; ++d)
#pragma unroll
            for (int r = 0; r < 16; ++r) o[d][r] *= alpha;
        }
        float ps = 0.f;
#pragma unroll
        for (int r = 0; r < 16; ++r) { s[r] = __builtin_amdgcn_exp2f(fmaf(s[r], SCALE_LOG2E, -m)); ps += s[r]; }
        l += ps;
        bf16x8 pb[2];
#pragma unroll
        for (int ks = 0; ks < 2; ++ks) {
          u32x4 u = {pack2(s[8 * ks], s[8 * ks + 1]), pack2(s[8 * ks + 2], s[8 * ks + 3]), pack2(s[8 * ks + 4], s[8 * ks + 5]), pack2(s[8 * ks + 6], s[8 * ks + 7])};
          pb[ks] = __builtin_bit_cast(bf16x8, u);
        }
#pragma unroll
        for (int d = 0; d < 4; ++d)
#pragma unroll
          for (int ks = 0; ks < 2; ++ks) {
            const s16x4 lo = __builtin_amdgcn_ds_read_tr16_b64_v4i16((LDS_AS s16x4*)(sm + sbase + (vaddr0 ^ (d << 6)) + ks * 4096));
            const s16x4 up = __builtin_amdgcn_ds_read_tr16_b64_v4i16((LDS_AS s16x4*)(sm + sbase + (vaddr0 ^ ((d << 6) | 32)) + ks * 4096 + 2048));
            const bf16x8 a = __builtin_shufflevector(lo, up, 0, 1, 2, 3, 4, 5, 6, 7);
            o[d] = __builtin_amdgcn_mfma_f32_32x32x16_bf16(a, pb[ks], o[d], 0, 0, 0);
          }
      }
    }
    cur_advance(cc, lens);
  }
  flush();
}

__global__ __launch_bounds__(256) void attn_combine(const int* __restrict__ lens, const float* __restrict__ po, const float* __restrict__ pml,
                                                    __hip_bfloat16* __restrict__ out) {
  const int p = blockIdx.x >> 2, qb = blockIdx.x & 3, d0 = threadIdx.x >> 6, lane = threadIdx.x & 63, r32 = lane & 31, hi = lane >> 5;
  const int b = p >> 3, h = p & 7;
  int tot = 0, P = 0, ntb = 1;
#pragma unroll
  for (int bb = 0; bb < B; ++bb) { const int nt = (eff_len(lens[bb]) + TILE - 1) / TILE; if (bb == b) { P = tot * HKV + h * nt; ntb = nt; } tot += nt; }
  const int T = tot * HKV;
  const int i0 = ((P + 1) * NWG + T - 1) / T - 1, i1 = ((P + ntb) * NWG + T - 1) / T - 1;
  float M = -__builtin_inff();
  for (int i = i0; i <= i1; ++i) { if (i * T / NWG == (i + 1) * T / NWG) continue;
    M = fmaxf(M, pml[((size_t)(i + p) * 128 + qb * 32 + r32) * 2]); }
  float L = 0.f; f32x4 acc[4] = {};
  for (int i = i0; i <= i1; ++i) { if (i * T / NWG == (i + 1) * T / NWG) continue;
    const f32x2 ml = *(const f32x2*)(pml + ((size_t)(i + p) * 128 + qb * 32 + r32) * 2);
    const float f = __builtin_amdgcn_exp2f(ml[0] - M); L += ml[1] * f;
    const float* src = po + (size_t)(i + p) * (128 * D) + (size_t)(qb * 4 + d0) * 1024 + lane * 4;
#pragma unroll
    for (int g = 0; g < 4; ++g) { const f32x4 x = *(const f32x4*)(src + g * 256); acc[g] += x * f; }
  }
  const float inv = 1.f / L;
  __hip_bfloat16* orow = out + ((size_t)((b * HQ + h * G) * SQ) + qb * 32 + r32) * D + 32 * d0 + 4 * hi;
#pragma unroll
  for (int g = 0; g < 4; ++g) { u32x2 u = {pack2(acc[g][0] * inv, acc[g][1] * inv), pack2(acc[g][2] * inv, acc[g][3] * inv)};
    *(u32x2*)(orow + 8 * g) = u; }
}

}

extern "C" void kernel_launch(void* const* d_in, const int* in_sizes, int n_in, void* d_out, int out_size, void* d_ws, size_t ws_size,
                              hipStream_t stream) {
  const __hip_bfloat16* q = (const __hip_bfloat16*)d_in[0];
  const __hip_bfloat16* kc = (const __hip_bfloat16*)d_in[1];
  const __hip_bfloat16* vc = (const __hip_bfloat16*)d_in[2];
  const int* bt = (const int*)d_in[3];
  const int* lens = (const int*)d_in[4];
  float* pml = (float*)d_ws;
  float* po = (float*)((char*)d_ws + (1u << 20));
  attn_split<<<NWG, 512, 0, stream>>>(q, kc, vc, bt, lens, po, pml);
  attn_combine<<<B * HKV * 4, 256, 0, stream>>>(lens, po, pml, (__hip_bfloat16*)d_out);
}
```

```cpp
#include <hip/hip_runtime.h>
#include <hip/hip_bf16.h>
#include <stdint.h>

namespace {

constexpr int B = 16, HQ = 32, HKV = 8, G = HQ / HKV, SQ = 32, D = 128, PAGE = 16, NBPS = 256, SKV = NBPS * PAGE;
constexpr int TILE = 64;
constexpr int NWG = 256;
constexpr int NS = 4;
constexpr int HALF_BYTES = 16384;
constexpr int STAGE_BYTES = 2 * HALF_BYTES;
constexpr float RESCALE_THR = 8.f;
constexpr int SCR_OFF = NS * STAGE_BYTES;
constexpr int SCR_ML = SCR_OFF + 16384;
constexpr int LDS_BYTES = SCR_ML + 1024;
constexpr float SCALE_LOG2E = 0.08838834764831845f * 1.4426950408889634f;

typedef short bf16x8 __attribute__((ext_vector_type(8)));
typedef short s16x4 __attribute__((ext_vector_type(4)));
typedef float f32x16 __attribute__((ext_vector_type(16)));
typedef float f32x4 __attribute__((ext_vector_type(4)));
typedef float f32x2 __attribute__((ext_vector_type(2)));
typedef __bf16 bfx2 __attribute__((ext_vector_type(2)));
typedef unsigned u32x4 __attribute__((ext_vector_type(4)));
typedef unsigned u32x2 __attribute__((ext_vector_type(2)));
#define LDS_AS __attribute__((address_space(3)))

__device__ __forceinline__ int eff_len(int l) { l = l > SKV ? SKV : l; return l <= 0 ? SKV : l; }
__device__ __forceinline__ unsigned swz(unsigned row) { return ((row & 3u) << 2) | ((row >> 2) & 3u); }
__device__ __forceinline__ float swap32_max(float x) {
  auto r = __builtin_amdgcn_permlane32_swap(__float_as_uint(x), __float_as_uint(x), false, false);
  return fmaxf(__uint_as_float(r[0]), __uint_as_float(r[1]));
}
__device__ __forceinline__ float swap32_sum(float x) {
  auto r = __builtin_amdgcn_permlane32_swap(__float_as_uint(x), __float_as_uint(x), false, false);
  return __uint_as_float(r[0]) + __uint_as_float(r[1]);
}
__device__ __forceinline__ unsigned pack2(float a, float b) {
  f32x2 f = {a, b};
  return __builtin_bit_cast(unsigned, __builtin_convertvector(f, bfx2));
}

template <bool NT>
__device__ __forceinline__ void dma16(const void* sbase, unsigned voff, unsigned lds_dst) {
  unsigned keep;
  if (NT)
    asm volatile("s_nop 4\n\ts_mov_b32 %0, m0\n\ts_mov_b32 m0, %3\n\ts_nop 0\n\tglobal_load_lds_dwordx4 %1, %2 nt\n\ts_mov_b32 m0, %0"
                 : "=&s"(keep) : "v"(voff), "s"(sbase), "s"(lds_dst) : "memory");
  else
    asm volatile("s_nop 4\n\ts_mov_b32 %0, m0\n\ts_mov_b32 m0, %3\n\ts_nop 0\n\tglobal_load_lds_dwordx4 %1, %2\n\ts_mov_b32 m0, %0"
                 : "=&s"(keep) : "v"(voff), "s"(sbase), "s"(lds_dst) : "memory");
}

struct Cur { int b, h, t, nt, len, isq; };

__device__ __forceinline__ void cur_advance(Cur& c, const int* lens) {
  if (c.isq) { c.isq = 0; return; }
  if (++c.t == c.nt) {
    c.t = 0; c.isq = 1;
    if (++c.h == HKV) {
      c.h = 0; ++c.b;
      const int bb = c.b < B ? c.b : B - 1;
      c.len = eff_len(lens[bb]); c.nt = (c.len + TILE - 1) / TILE;
    }
  }
}

#define WAITBAR(n) asm volatile("s_waitcnt vmcnt(" #n ")\n\ts_barrier" ::: "memory")
#define LBAR() asm volatile("s_waitcnt lgkmcnt(0)\n\ts_barrier" ::: "memory")

__global__ __launch_bounds__(512, 2) void attn_split(const __hip_bfloat16* __restrict__ q, const __hip_bfloat16* __restrict__ kc,
                                                     const __hip_bfloat16* __restrict__ vc, const int* __restrict__ bt,
                                                     const int* __restrict__ lens, float* __restrict__ po, float* __restrict__ pml) {
  __shared__ __attribute__((aligned(256))) char smem[LDS_BYTES];
  LDS_AS char* const sm = (LDS_AS char*)smem;
  const unsigned sm0 = (unsigned)(size_t)sm;
  const int tid = threadIdx.x, lane = tid & 63;
  const int w = __builtin_amdgcn_readfirstlane(tid >> 6);
  const int wg = blockIdx.x;
  const int r32 = lane & 31, hi = lane >> 5, gsel = w >> 2, qb = w & 3;

  int tot = 0;
#pragma unroll
  for (int b = 0; b < B; ++b) tot += (eff_len(lens[b]) + TILE - 1) / TILE;
  const int T = tot * HKV;
  const int g0 = wg * T / NWG, g1 = (wg + 1) * T / NWG;
  if (g0 == g1) return;
  Cur start; { int acc = 0, sb = 0, sP = 0, snt = 1, sl = 1;
#pragma unroll
    for (int b = 0; b < B; ++b) { const int l = eff_len(lens[b]), nt = (l + TILE - 1) / TILE;
      if (acc <= g0) { sb = b; sP = acc; snt = nt; sl = l; }
      acc += nt * HKV; }
    start.b = sb; start.h = (g0 - sP) / snt; start.t = (g0 - sP) % snt; start.nt = snt; start.len = sl; start.isq = 1; }

  unsigned vkv[2], vq[2];
#pragma unroll
  for (int i = 0; i < 2; ++i) { const unsigned rt = 8 * w + 4 * i + (lane >> 4), rp = (w & 1) * 8 + 4 * i + (lane >> 4);
    const unsigned ch = (((unsigned)lane & 15u) ^ swz(rt)) * 16u;
    vkv[i] = rp * 2048u + ch; vq[i] = rt * 256u + ch; }

  auto issue = [&](const Cur& c, int slot, int part) {
    const unsigned ldsb = sm0 + (unsigned)(part * NS + slot) * HALF_BYTES + (unsigned)(8 * w) * 256u;
    if (c.isq) {
      const char* qbase = (const char*)q + (size_t)((c.b * HQ + c.h * G) * SQ + part * 64) * (D * 2);
#pragma unroll
      for (int i = 0; i < 2; ++i) dma16<false>(qbase, vq[i], ldsb + i * 1024u);
    } else {
      const int phys = bt[c.b * NBPS + c.t * 4 + (w >> 1)];
      const char* base = (const char*)(part ? vc : kc) + (size_t)phys * (PAGE * HKV * D * 2) + (size_t)c.h * (D * 2);
#pragma unroll
      for (int i = 0; i < 2; ++i) dma16<true>(base, vkv[i], ldsb + i * 1024u);
    }
  };

  bf16x8 qf[8];
  f32x16 o[4], sc;
#pragma unroll
  for (int d = 0; d < 4; ++d) o[d] = f32x16{};
  float m = -__builtin_inff(), l = 0.f;
  int fb = 0, fh = 0;

  auto flush = [&]() {
    const float lf = swap32_sum(l);
    int lane = threadIdx.x & 63; asm volatile("" : "+v"(lane));
    const int r32 = lane & 31, hi = lane >> 5;
    LDS_AS float* const scr = (LDS_AS float*)(sm + SCR_OFF) + qb * 1024;
    LDS_AS float* const sml = (LDS_AS float*)(sm + SCR_ML) + (qb * 32 + r32) * 2;
    if (gsel == 1 && hi == 0) { sml[0] = m; sml[1] = lf; }
    float fA = 1.f, fB = 0.f, M = m, L = lf;
#pragma unroll
    for (int d = 0; d < 4; ++d) {
      if (gsel == 1) {
#pragma unroll
        for (int g = 0; g < 4; ++g) *(LDS_AS f32x4*)(scr + (g * 64 + lane) * 4) = f32x4{o[d][4 * g], o[d][4 * g + 1], o[d][4 * g + 2], o[d][4 * g + 3]};
      }
      LBAR();
      if (gsel == 0) {
        if (d == 0) { const float mB = sml[0], lB = sml[1]; M = fmaxf(m, mB);
          fA = __builtin_amdgcn_exp2f(m - M); fB = __builtin_amdgcn_exp2f(mB - M); L = lf * fA + lB * fB; }
#pragma unroll
        for (int g = 0; g < 4; ++g) { const f32x4 x = *(LDS_AS f32x4*)(scr + (g * 64 + lane) * 4);
#pragma unroll
          for (int e = 0; e < 4; ++e) o[d][4 * g + e] = o[d][4 * g + e] * fA + x[e] * fB; }
      }
      LBAR();
    }
    if (gsel == 0) {
      const int slot = wg + fb * HKV + fh;
      float* dst = po + (size_t)slot * (128 * D) + (size_t)(qb * 4) * 1024 + lane * 4;
#pragma unroll
      for (int d = 0; d < 4; ++d)
#pragma unroll
        for (int g = 0; g < 4; ++g) *(f32x4*)(dst + (d * 4 + g) * 256) = f32x4{o[d][4 * g], o[d][4 * g + 1], o[d][4 * g + 2], o[d][4 * g + 3]};
      if (hi == 0) *(f32x2*)(pml + ((size_t)slot * 128 + qb * 32 + r32) * 2) = f32x2{M, L};
    }
#pragma unroll
    for (int d = 0; d < 4; ++d) o[d] = f32x16{};
    m = -__builtin_inff(); l = 0.f;
  };

  const unsigned krow = 32 * gsel + r32;
  const unsigned kaddr0 = krow * 256u + (((unsigned)hi) ^ swz(krow)) * 16u;
  const unsigned tq = (lane & 15) >> 2, tp = lane & 3, tblk = (lane >> 4) & 1;
  const unsigned vaddr0 = (32 * gsel + 4 * hi + tq) * 256u + ((2 * tblk + (tp >> 1)) ^ ((tq << 2) | (unsigned)hi)) * 16u + 8u * (tp & 1);
  const unsigned qrow = 32 * (qb & 1) + r32;
  const unsigned qaddr0 = (unsigned)((qb >> 1) * NS) * HALF_BYTES + qrow * 256u + (((unsigned)hi) ^ swz(qrow)) * 16u;

  auto qk = [&](int slot) -> f32x16 {
    const unsigned kb = (unsigned)slot * HALF_BYTES + kaddr0;
    bf16x8 a[8];
#pragma unroll
    for (int ks = 0; ks < 8; ++ks) a[ks] = *(LDS_AS bf16x8*)(sm + (kb ^ (ks << 5)));
    f32x16 s = f32x16{};
#pragma unroll
    for (int ks = 0; ks < 8; ++ks) s = __builtin_amdgcn_mfma_f32_32x32x16_bf16(a[ks], qf[ks], s, 0, 0, 0);
    return s;
  };
  auto softmax_pv = [&](f32x16& s, int nvalid, int slot) {
    if (nvalid < 32) {
      const int nvh = nvalid - 4 * hi;
#pragma unroll
      for (int r = 0; r < 16; ++r) s[r] = (r & 3) + 8 * (r >> 2) < nvh ? s[r] : -__builtin_inff();
    }
    float tmax = s[0];
#pragma unroll
    for (int r = 1; r < 16; ++r) tmax = fmaxf(tmax, s[r]);
    tmax = swap32_max(tmax) * SCALE_LOG2E;
    if (__any(tmax > m + RESCALE_THR)) {
      const float mn = fmaxf(m, tmax), alpha = __builtin_amdgcn_exp2f(m - mn);
      m = mn; l *= alpha;
#pragma unroll
      for (int d = 0; d < 4; ++d)
#pragma unroll
        for (int r = 0; r < 16; ++r) o[d][r] *= alpha;
    }
    const float msub = m == -__builtin_inff() ? 0.f : m;
    float ps = 0.f;
#pragma unroll
    for (int r = 0; r < 16; ++r) { s[r] = __builtin_amdgcn_exp2f(fmaf(s[r], SCALE_LOG2E, -msub)); ps += s[r]; }
    l += ps;
    bf16x8 pb[2];
#pragma unroll
    for (int ks = 0; ks < 2; ++ks) {
      u32x4 u = {pack2(s[8 * ks], s[8 * ks + 1]), pack2(s[8 * ks + 2], s[8 * ks + 3]), pack2(s[8 * ks + 4], s[8 * ks + 5]), pack2(s[8 * ks + 6], s[8 * ks + 7])};
      pb[ks] = __builtin_bit_cast(bf16x8, u);
    }
    const unsigned vb = (unsigned)(NS + slot) * HALF_BYTES + vaddr0;
#pragma unroll
    for (int d = 0; d < 4; ++d)
#pragma unroll
      for (int ks = 0; ks < 2; ++ks) {
        const s16x4 lo = __builtin_amdgcn_ds_read_tr16_b64_v4i16((LDS_AS s16x4*)(sm + (vb ^ (d << 6)) + ks * 4096));
        const s16x4 up = __builtin_amdgcn_ds_read_tr16_b64_v4i16((LDS_AS s16x4*)(sm + (vb ^ ((d << 6) | 32)) + ks * 4096 + 2048));
        const bf16x8 a = __builtin_shufflevector(lo, up, 0, 1, 2, 3, 4, 5, 6, 7);
        o[d] = __builtin_amdgcn_mfma_f32_32x32x16_bf16(a, pb[ks], o[d], 0, 0, 0);
      }
  };

  Cur pk = start, pv = start, cc = start;
  int remK = g1 - g0, remV = g1 - g0;
  int ik = 0, iv = 0, nops = 0;
  auto stepK = [&]() { if (remK > 0) { issue(pk, ik % NS, 0); ++ik; nops += 2; if (!pk.isq) --remK; cur_advance(pk, lens); } };
  auto stepV = [&]() { if (remV > 0) { issue(pv, iv % NS, 1); ++iv; nops += 2; if (!pv.isq) --remV; cur_advance(pv, lens); } };
  stepK();
  for (int p = 0; p < NS - 1; ++p) { stepK(); stepV(); }
  bool first = true;
  for (int i = 0; i < iv; ++i) {
    const int need = 2 + 4 * (i + 1), after = nops > need ? nops - need : 0;
    if (after >= 8) WAITBAR(8); else if (after == 6) WAITBAR(6); else if (after == 4) WAITBAR(4); else if (after == 2) WAITBAR(2); else WAITBAR(0);
    static_assert(NS == 4, "wait table");
    const bool has_next = i + 1 < ik;
    if (cc.isq) {
      if (!first) flush();
      first = false; fb = cc.b; fh = cc.h;
      const bool zq = lens[cc.b] <= 0;
      const unsigned qs = (unsigned)(i % NS) * HALF_BYTES + qaddr0;
#pragma unroll
      for (int s = 0; s < 8; ++s) { qf[s] = *(LDS_AS bf16x8*)(sm + (qs ^ (s << 5))); if (zq) qf[s] = bf16x8{}; }
      LBAR();
      stepK(); stepV();
      sc = qk((i + 1) % NS);
    } else {
      const int nvalid = cc.len - cc.t * TILE - 32 * gsel;
      Cur nx = cc; cur_advance(nx, lens);
      stepK();
      if (has_next && !nx.isq) {
        f32x16 sn = qk((i + 1) % NS);
        stepV();
        softmax_pv(sc, nvalid, i % NS);
        sc = sn;
      } else {
        stepV();
        softmax_pv(sc, nvalid, i % NS);
      }
    }
    cur_advance(cc, lens);
  }
  flush();
}

__global__ __launch_bounds__(256) void attn_combine(const int* __restrict__ lens, const float* __restrict__ po, const float* __restrict__ pml,
                                                    __hip_bfloat16* __restrict__ out) {
  const int p = blockIdx.x >> 2, qb = blockIdx.x & 3, d0 = threadIdx.x >> 6, lane = threadIdx.x & 63, r32 = lane & 31, hi = lane >> 5;
  const int b = p >> 3, h = p & 7;
  int tot = 0, P = 0, ntb = 1;
#pragma unroll
  for (int bb = 0; bb < B; ++bb) { const int nt = (eff_len(lens[bb]) + TILE - 1) / TILE; if (bb == b) { P = tot * HKV + h * nt; ntb = nt; } tot += nt; }
  const int T = tot * HKV;
  const int i0 = ((P + 1) * NWG + T - 1) / T - 1, i1 = ((P + ntb) * NWG + T - 1) / T - 1;
  float M = -__builtin_inff();
  for (int i = i0; i <= i1; ++i) { if (i * T / NWG == (i + 1) * T / NWG) continue;
    M = fmaxf(M, pml[((size_t)(i + p) * 128 + qb * 32 + r32) * 2]); }
  float L = 0.f; f32x4 acc[4] = {};
  for (int i = i0; i <= i1; ++i) { if (i * T / NWG == (i + 1) * T / NWG) continue;
    const f32x2 ml = *(const f32x2*)(pml + ((size_t)(i + p) * 128 + qb * 32 + r32) * 2);
    const float f = __builtin_amdgcn_exp2f(ml[0] - M); L += ml[1] * f;
    const float* src = po + (size_t)(i + p) * (128 * D) + (size_t)(qb * 4 + d0) * 1024 + lane * 4;
#pragma unroll
    for (int g = 0; g < 4; ++g) { const f32x4 x = *(const f32x4*)(src + g * 256); acc[g] += x * f; }
  }
  const float inv = 1.f / L;
  __hip_bfloat16* orow = out + ((size_t)((b * HQ + h * G) * SQ) + qb * 32 + r32) * D + 32 * d0 + 4 * hi;
#pragma unroll
  for (int g = 0; g < 4; ++g) { u32x2 u = {pack2(acc[g][0] * inv, acc[g][1] * inv), pack2(acc[g][2] * inv, acc[g][3] * inv)};
    *(u32x2*)(orow + 8 * g) = u; }
}

}

extern "C" void kernel_launch(void* const* d_in, const int* in_sizes, int n_in, void* d_out, int out_size, void* d_ws, size_t ws_size,
                              hipStream_t stream) {
  const __hip_bfloat16* q = (const __hip_bfloat16*)d_in[0];
  const __hip_bfloat16* kc = (const __hip_bfloat16*)d_in[1];
  const __hip_bfloat16* vc = (const __hip_bfloat16*)d_in[2];
  const int* bt = (const int*)d_in[3];
  const int* lens = (const int*)d_in[4];
  float* pml = (float*)d_ws;
  float* po = (float*)((char*)d_ws + (1u << 20));
  attn_split<<<NWG, 512, 0, stream>>>(q, kc, vc, bt, lens, po, pml);
  attn_combine<<<B * HKV * 4, 256, 0, stream>>>(lens, po, pml, (__hip_bfloat16*)d_out);
}
```

```cpp
#include <hip/hip_runtime.h>
#include <hip/hip_bf16.h>
#include <stdint.h>

namespace {

constexpr int B = 16, HQ = 32, HKV = 8, G = HQ / HKV, SQ = 32, D = 128, PAGE = 16, NBPS = 256, SKV = NBPS * PAGE;
constexpr int TILE = 64;
constexpr int NWG = 256;
constexpr int NS = 4;
constexpr int STAGE_BYTES = 32768;
constexpr int SCR_OFF = NS * STAGE_BYTES;
constexpr int SCR_ML = SCR_OFF + 16384;
constexpr int LDS_BYTES = SCR_ML + 1024;
constexpr float SCALE_LOG2E = 0.08838834764831845f * 1.4426950408889634f;

typedef short bf16x8 __attribute__((ext_vector_type(8)));
typedef short s16x4 __attribute__((ext_vector_type(4)));
typedef float f32x16 __attribute__((ext_vector_type(16)));
typedef float f32x4 __attribute__((ext_vector_type(4)));
typedef float f32x2 __attribute__((ext_vector_type(2)));
typedef __bf16 bfx2 __attribute__((ext_vector_type(2)));
typedef unsigned u32x4 __attribute__((ext_vector_type(4)));
typedef unsigned u32x2 __attribute__((ext_vector_type(2)));
#define LDS_AS __attribute__((address_space(3)))

__device__ __forceinline__ int eff_len(int l) { l = l > SKV ? SKV : l; return l <= 0 ? SKV : l; }
__device__ __forceinline__ unsigned swz(unsigned row) { return ((row & 3u) << 2) | ((row >> 2) & 3u); }
__device__ __forceinline__ float swap32_max(float x) {
  auto r = __builtin_amdgcn_permlane32_swap(__float_as_uint(x), __float_as_uint(x), false, false);
  return fmaxf(__uint_as_float(r[0]), __uint_as_float(r[1]));
}
__device__ __forceinline__ float swap32_sum(float x) {
  auto r = __builtin_amdgcn_permlane32_swap(__float_as_uint(x), __float_as_uint(x), false, false);
  return __uint_as_float(r[0]) + __uint_as_float(r[1]);
}
__device__ __forceinline__ unsigned pack2(float a, float b) {
  f32x2 f = {a, b};
  return __builtin_bit_cast(unsigned, __builtin_convertvector(f, bfx2));
}

template <bool NT>
__device__ __forceinline__ void dma16(const void* sbase, unsigned voff, unsigned lds_dst) {
  unsigned keep;
  if (NT)
    asm volatile("s_nop 4\n\ts_mov_b32 %0, m0\n\ts_mov_b32 m0, %3\n\ts_nop 0\n\tglobal_load_lds_dwordx4 %1, %2 nt\n\ts_mov_b32 m0, %0"
                 : "=&s"(keep) : "v"(voff), "s"(sbase), "s"(lds_dst) : "memory");
  else
    asm volatile("s_nop 4\n\ts_mov_b32 %0, m0\n\ts_mov_b32 m0, %3\n\ts_nop 0\n\tglobal_load_lds_dwordx4 %1, %2\n\ts_mov_b32 m0, %0"
                 : "=&s"(keep) : "v"(voff), "s"(sbase), "s"(lds_dst) : "memory");
}

struct Cur { int b, h, t, nt, len, isq; };

__device__ __forceinline__ void cur_advance(Cur& c, const int* lens) {
  if (c.isq) { c.isq = 0; return; }
  if (++c.t == c.nt) {
    c.t = 0; c.isq = 1;
    if (++c.h == HKV) {
      c.h = 0; ++c.b;
      const int bb = c.b < B ? c.b : B - 1;
      c.len = eff_len(lens[bb]); c.nt = (c.len + TILE - 1) / TILE;
    }
  }
}

#define WAITBAR(n) asm volatile("s_waitcnt vmcnt(" #n ")\n\ts_barrier" ::: "memory")
#define LBAR() asm volatile("s_waitcnt lgkmcnt(0)\n\ts_barrier" ::: "memory")

__global__ __launch_bounds__(512, 2) void attn_split(const __hip_bfloat16* __restrict__ q, const __hip_bfloat16* __restrict__ kc,
                                                     const __hip_bfloat16* __restrict__ vc, const int* __restrict__ bt,
                                                     const int* __restrict__ lens, float* __restrict__ po, float* __restrict__ pml) {
  __shared__ __attribute__((aligned(256))) char smem[LDS_BYTES];
  LDS_AS char* const sm = (LDS_AS char*)smem;
  const unsigned sm0 = (unsigned)(size_t)sm;
  const int tid = threadIdx.x, lane = tid & 63;
  const int w = __builtin_amdgcn_readfirstlane(tid >> 6);
  const int wg = blockIdx.x;
  const int r32 = lane & 31, hi = lane >> 5, gsel = w >> 2, qb = w & 3;

  int tot = 0;
#pragma unroll
  for (int b = 0; b < B; ++b) tot += (eff_len(lens[b]) + TILE - 1) / TILE;
  const int T = tot * HKV;
  const int g0 = wg * T / NWG, g1 = (wg + 1) * T / NWG;
  if (g0 == g1) return;
  Cur start; { int acc = 0, sb = 0, sP = 0, snt = 1, sl = 1;
#pragma unroll
    for (int b = 0; b < B; ++b) { const int l = eff_len(lens[b]), nt = (l + TILE - 1) / TILE;
      if (acc <= g0) { sb = b; sP = acc; snt = nt; sl = l; }
      acc += nt * HKV; }
    start.b = sb; start.h = (g0 - sP) / snt; start.t = (g0 - sP) % snt; start.nt = snt; start.len = sl; start.isq = 1; }

  unsigned vkv[2];
#pragma unroll
  for (int i = 0; i < 2; ++i) { const unsigned rt = 8 * w + 4 * i + (lane >> 4), rp = (w & 1) * 8 + 4 * i + (lane >> 4);
    vkv[i] = rp * 2048u + (((unsigned)lane & 15u) ^ swz(rt)) * 16u; }

  auto issue = [&](const Cur& c, int slot, int part) {
    const unsigned ldsb = sm0 + (unsigned)slot * STAGE_BYTES;
    if (c.isq) {
      const char* qbase = (const char*)q + (size_t)((c.b * HQ + c.h * G) * SQ) * (D * 2);
#pragma unroll
      for (int i = 2 * part; i < 2 * part + 2; ++i) { const unsigned row = 16 * w + 4 * i + (lane >> 4);
        dma16<false>(qbase, row * 256u + (((unsigned)lane & 15u) ^ swz(row)) * 16u, ldsb + (16 * w + 4 * i) * 256u); }
    } else {
      const int phys = bt[c.b * NBPS + c.t * 4 + (w >> 1)];
      const size_t poff = (size_t)phys * (PAGE * HKV * D * 2) + (size_t)c.h * (D * 2);
      const char* kb = (const char*)(part ? vc : kc) + poff;
#pragma unroll
      for (int i = 0; i < 2; ++i) dma16<true>(kb, vkv[i], ldsb + part * 16384u + (8 * w + 4 * i) * 256u);
    }
  };

  bf16x8 qf[8];
  f32x16 o[4];
#pragma unroll
  for (int d = 0; d < 4; ++d) o[d] = f32x16{};
  float m = -__builtin_inff(), l = 0.f;
  int fb = 0, fh = 0;

  auto flush = [&]() {
    const float lf = swap32_sum(l);
    LDS_AS float* const scr = (LDS_AS float*)(sm + SCR_OFF) + qb * 1024;
    LDS_AS float* const sml = (LDS_AS float*)(sm + SCR_ML) + (qb * 32 + r32) * 2;
    if (gsel == 1 && hi == 0) { sml[0] = m; sml[1] = lf; }
    float fA = 1.f, fB = 0.f, M = m, L = lf;
#pragma unroll
    for (int d = 0; d < 4; ++d) {
      if (gsel == 1) {
#pragma unroll
        for (int g = 0; g < 4; ++g) *(LDS_AS f32x4*)(scr + (g * 64 + lane) * 4) = f32x4{o[d][4 * g], o[d][4 * g + 1], o[d][4 * g + 2], o[d][4 * g + 3]};
      }
      LBAR();
      if (gsel == 0) {
        if (d == 0) { const float mB = sml[0], lB = sml[1]; M = fmaxf(m, mB);
          fA = __builtin_amdgcn_exp2f(m - M); fB = __builtin_amdgcn_exp2f(mB - M); L = lf * fA + lB * fB; }
#pragma unroll
        for (int g = 0; g < 4; ++g) { const f32x4 x = *(LDS_AS f32x4*)(scr + (g * 64 + lane) * 4);
#pragma unroll
          for (int e = 0; e < 4; ++e) o[d][4 * g + e] = o[d][4 * g + e] * fA + x[e] * fB; }
      }
      LBAR();
    }
    if (gsel == 0) {
      const int slot = wg + fb * HKV + fh;
      float* dst = po + (size_t)slot * (128 * D) + (size_t)(qb * 4) * 1024 + lane * 4;
#pragma unroll
      for (int d = 0; d < 4; ++d)
#pragma unroll
        for (int g = 0; g < 4; ++g) *(f32x4*)(dst + (d * 4 + g) * 256) = f32x4{o[d][4 * g], o[d][4 * g + 1], o[d][4 * g + 2], o[d][4 * g + 3]};
      if (hi == 0) *(f32x2*)(pml + ((size_t)slot * 128 + qb * 32 + r32) * 2) = f32x2{M, L};
    }
#pragma unroll
    for (int d = 0; d < 4; ++d) o[d] = f32x16{};
    m = -__builtin_inff(); l = 0.f;
  };

  const unsigned krow = 32 * gsel + r32;
  const unsigned kaddr0 = krow * 256u + (((unsigned)hi) ^ swz(krow)) * 16u;
  const unsigned tq = (lane & 15) >> 2, tp = lane & 3, tblk = (lane >> 4) & 1;
  const unsigned vaddr0 = 16384u + (32 * gsel + 4 * hi + tq) * 256u + ((2 * tblk + (tp >> 1)) ^ ((tq << 2) | (unsigned)hi)) * 16u + 8u * (tp & 1);
  const unsigned qrow = 32 * qb + r32;
  const unsigned qaddr0 = qrow * 256u + (((unsigned)hi) ^ swz(qrow)) * 16u;

  Cur pc = start, cc = start;
  int prem = g1 - g0;
  int issued = 0;
  for (int s = 0; s < NS - 1 && prem > 0; ++s) { issue(pc, s, 0); issue(pc, s, 1); ++issued; if (!pc.isq) --prem; cur_advance(pc, lens); }
  bool first = true;
  for (int i = 0; i < issued; ++i) {
    const int ahead = issued - i - 1;
    if (ahead >= 2) WAITBAR(8); else if (ahead == 1) WAITBAR(4); else WAITBAR(0);
    static_assert(NS == 4, "wait table");
    if (cc.isq && !first) flush();
    const bool doissue = prem > 0; const Cur pcur = pc;
    if (doissue) { issue(pcur, (i + NS - 1) % NS, 0); ++issued; if (!pc.isq) --prem; cur_advance(pc, lens); }
    const unsigned sbase = (unsigned)(i % NS) * STAGE_BYTES;
    if (cc.isq) {
      first = false; fb = cc.b; fh = cc.h;
      const bool zq = lens[cc.b] <= 0;
#pragma unroll
      for (int s = 0; s < 8; ++s) { qf[s] = *(LDS_AS bf16x8*)(sm + sbase + (qaddr0 ^ (s << 5))); if (zq) qf[s] = bf16x8{}; }
      if (doissue) issue(pcur, (i + NS - 1) % NS, 1);
    } else {
      const int nvalid = cc.len - cc.t * TILE - 32 * gsel;
      if (nvalid <= 0) { if (doissue) issue(pcur, (i + NS - 1) % NS, 1); }
      if (nvalid > 0) {
        f32x16 s = f32x16{};
        bf16x8 a[8]; const unsigned kb0 = sbase + kaddr0;
#pragma unroll
        for (int ks = 0; ks < 8; ++ks) a[ks] = *(LDS_AS bf16x8*)(sm + (kb0 ^ (ks << 5)));
        __builtin_amdgcn_sched_barrier(0);
#pragma unroll
        for (int ks = 0; ks < 8; ++ks) s = __builtin_amdgcn_mfma_f32_32x32x16_bf16(a[ks], qf[ks], s, 0, 0, 0);
        if (doissue) issue(pcur, (i + NS - 1) % NS, 1);
        if (nvalid < 32) {
#pragma unroll
          for (int r = 0; r < 16; ++r) s[r] = (r & 3) + 8 * (r >> 2) < nvalid - 4 * hi ? s[r] : -__builtin_inff();
        }
        float tmax = s[0];
#pragma unroll
        for (int r = 1; r < 16; ++r) tmax = fmaxf(tmax, s[r]);
        tmax = swap32_max(tmax) * SCALE_LOG2E;
        if (__any(tmax > m + 8.f)) {
          const float mn = fmaxf(m, tmax), alpha = __builtin_amdgcn_exp2f(m - mn);
          m = mn; l *= alpha;
#pragma unroll
          for (int d = 0; d < 4; ++d)
#pragma unroll
            for (int r = 0; r < 16; ++r) o[d][r] *= alpha;
        }
        float ps = 0.f;
#pragma unroll
        for (int r = 0; r < 16; ++r) { s[r] = __builtin_amdgcn_exp2f(fmaf(s[r], SCALE_LOG2E, -m)); ps += s[r]; }
        l += ps;
        bf16x8 pb[2];
#pragma unroll
        for (int ks = 0; ks < 2; ++ks) {
          u32x4 u = {pack2(s[8 * ks], s[8 * ks + 1]), pack2(s[8 * ks + 2], s[8 * ks + 3]), pack2(s[8 * ks + 4], s[8 * ks + 5]), pack2(s[8 * ks + 6], s[8 * ks + 7])};
          pb[ks] = __builtin_bit_cast(bf16x8, u);
        }
        const unsigned vb0 = sbase + vaddr0;
#pragma unroll
        for (int d = 0; d < 4; ++d)
#pragma unroll
          for (int ks = 0; ks < 2; ++ks) {
            const s16x4 lo = __builtin_amdgcn_ds_read_tr16_b64_v4i16((LDS_AS s16x4*)(sm + (vb0 ^ (d << 6)) + ks * 4096));
            const s16x4 up = __builtin_amdgcn_ds_read_tr16_b64_v4i16((LDS_AS s16x4*)(sm + (vb0 ^ ((d << 6) | 32)) + ks * 4096 + 2048));
            const bf16x8 a = __builtin_shufflevector(lo, up, 0, 1, 2, 3, 4, 5, 6, 7);
            o[d] = __builtin_amdgcn_mfma_f32_32x32x16_bf16(a, pb[ks], o[d], 0, 0, 0);
          }
      }
    }
    cur_advance(cc, lens);
  }
  flush();
}

__global__ __launch_bounds__(256) void attn_combine(const int* __restrict__ lens, const float* __restrict__ po, const float* __restrict__ pml,
                                                    __hip_bfloat16* __restrict__ out) {
  const int p = blockIdx.x >> 2, qb = blockIdx.x & 3, d0 = threadIdx.x >> 6, lane = threadIdx.x & 63, r32 = lane & 31, hi = lane >> 5;
  const int b = p >> 3, h = p & 7;
  int tot = 0, P = 0, ntb = 1;
#pragma unroll
  for (int bb = 0; bb < B; ++bb) { const int nt = (eff_len(lens[bb]) + TILE - 1) / TILE; if (bb == b) { P = tot * HKV + h * nt; ntb = nt; } tot += nt; }
  const int T = tot * HKV;
  const int i0 = ((P + 1) * NWG + T - 1) / T - 1, i1 = ((P + ntb) * NWG + T - 1) / T - 1;
  float M = -__builtin_inff();
  for (int i = i0; i <= i1; ++i) { if (i * T / NWG == (i + 1) * T / NWG) continue;
    M = fmaxf(M, pml[((size_t)(i + p) * 128 + qb * 32 + r32) * 2]); }
  float L = 0.f; f32x4 acc[4] = {};
  for (int i = i0; i <= i1; ++i) { if (i * T / NWG == (i + 1) * T / NWG) continue;
    const f32x2 ml = *(const f32x2*)(pml + ((size_t)(i + p) * 128 + qb * 32 + r32) * 2);
    const float f = __builtin_amdgcn_exp2f(ml[0] - M); L += ml[1] * f;
    const float* src = po + (size_t)(i + p) * (128 * D) + (size_t)(qb * 4 + d0) * 1024 + lane * 4;
#pragma unroll
    for (int g = 0; g < 4; ++g) { const f32x4 x = *(const f32x4*)(src + g * 256); acc[g] += x * f; }
  }
  const float inv = 1.f / L;
  __hip_bfloat16* orow = out + ((size_t)((b * HQ + h * G) * SQ) + qb * 32 + r32) * D + 32 * d0 + 4 * hi;
#pragma unroll
  for (int g = 0; g < 4; ++g) { u32x2 u = {pack2(acc[g][0] * inv, acc[g][1] * inv), pack2(acc[g][2] * inv, acc[g][3] * inv)};
    *(u32x2*)(orow + 8 * g) = u; }
}

}

extern "C" void kernel_launch(void* const* d_in, const int* in_sizes, int n_in, void* d_out, int out_size, void* d_ws, size_t ws_size,
                              hipStream_t stream) {
  const __hip_bfloat16* q = (const __hip_bfloat16*)d_in[0];
  const __hip_bfloat16* kc = (const __hip_bfloat16*)d_in[1];
  const __hip_bfloat16* vc = (const __hip_bfloat16*)d_in[2];
  const int* bt = (const int*)d_in[3];
  const int* lens = (const int*)d_in[4];
  float* pml = (float*)d_ws;
  float* po = (float*)((char*)d_ws + (1u << 20));
  attn_split<<<NWG, 512, 0, stream>>>(q, kc, vc, bt, lens, po, pml);
  attn_combine<<<B * HKV * 4, 256, 0, stream>>>(lens, po, pml, (__hip_bfloat16*)d_out);
}
```

```cpp
#include <hip/hip_runtime.h>
#include <hip/hip_bf16.h>
#include <stdint.h>

namespace {

constexpr int B = 16, HQ = 32, HKV = 8, G = HQ / HKV, SQ = 32, D = 128, PAGE = 16, NBPS = 256, SKV = NBPS * PAGE;
constexpr int TILE = 64;
constexpr int NWG = 256;
constexpr int NS = 4;
constexpr int STAGE_BYTES = 32768;
constexpr int SCR_OFF = NS * STAGE_BYTES;
constexpr int SCR_ML = SCR_OFF + 16384;
constexpr int LDS_BYTES = SCR_ML + 1024;
constexpr float SCALE_LOG2E = 0.08838834764831845f * 1.4426950408889634f;

typedef short bf16x8 __attribute__((ext_vector_type(8)));
typedef short s16x4 __attribute__((ext_vector_type(4)));
typedef float f32x16 __attribute__((ext_vector_type(16)));
typedef float f32x4 __attribute__((ext_vector_type(4)));
typedef float f32x2 __attribute__((ext_vector_type(2)));
typedef __bf16 bfx2 __attribute__((ext_vector_type(2)));
typedef unsigned u32x4 __attribute__((ext_vector_type(4)));
typedef unsigned u32x2 __attribute__((ext_vector_type(2)));
#define LDS_AS __attribute__((address_space(3)))

__device__ __forceinline__ int eff_len(int l) { l = l > SKV ? SKV : l; return l <= 0 ? SKV : l; }
__device__ __forceinline__ unsigned swz(unsigned row) { return ((row & 3u) << 2) | ((row >> 2) & 3u); }
__device__ __forceinline__ float swap32_max(float x) {
  auto r = __builtin_amdgcn_permlane32_swap(__float_as_uint(x), __float_as_uint(x), false, false);
  return fmaxf(__uint_as_float(r[0]), __uint_as_float(r[1]));
}
__device__ __forceinline__ float swap32_sum(float x) {
  auto r = __builtin_amdgcn_permlane32_swap(__float_as_uint(x), __float_as_uint(x), false, false);
  return __uint_as_float(r[0]) + __uint_as_float(r[1]);
}
__device__ __forceinline__ unsigned pack2(float a, float b) {
  f32x2 f = {a, b};
  return __builtin_bit_cast(unsigned, __builtin_convertvector(f, bfx2));
}

template <bool NT>
__device__ __forceinline__ void dma16(const void* sbase, unsigned voff, unsigned lds_dst) {
  unsigned keep;
  if (NT)
    asm volatile("s_nop 4\n\ts_mov_b32 %0, m0\n\ts_mov_b32 m0, %3\n\ts_nop 0\n\tglobal_load_lds_dwordx4 %1, %2 nt\n\ts_mov_b32 m0, %0"
                 : "=&s"(keep) : "v"(voff), "s"(sbase), "s"(lds_dst) : "memory");
  else
    asm volatile("s_nop 4\n\ts_mov_b32 %0, m0\n\ts_mov_b32 m0, %3\n\ts_nop 0\n\tglobal_load_lds_dwordx4 %1, %2\n\ts_mov_b32 m0, %0"
                 : "=&s"(keep) : "v"(voff), "s"(sbase), "s"(lds_dst) : "memory");
}

struct Cur { int b, h, t, nt, len, isq; };

__device__ __forceinline__ void cur_advance(Cur& c, const int* lens) {
  if (c.isq) { c.isq = 0; return; }
  if (++c.t == c.nt) {
    c.t = 0; c.isq = 1;
    if (++c.h == HKV) {
      c.h = 0; ++c.b;
      const int bb = c.b < B ? c.b : B - 1;
      c.len = eff_len(lens[bb]); c.nt = (c.len + TILE - 1) / TILE;
    }
  }
}

#define WAITBAR(n) asm volatile("s_waitcnt vmcnt(" #n ")\n\ts_barrier" ::: "memory")
#define LBAR() asm volatile("s_waitcnt lgkmcnt(0)\n\ts_barrier" ::: "memory")

__global__ __launch_bounds__(512, 2) void attn_split(const __hip_bfloat16* __restrict__ q, const __hip_bfloat16* __restrict__ kc,
                                                     const __hip_bfloat16* __restrict__ vc, const int* __restrict__ bt,
                                                     const int* __restrict__ lens, float* __restrict__ po, float* __restrict__ pml) {
  __shared__ __attribute__((aligned(256))) char smem[NS * STAGE_BYTES];
  LDS_AS char* const sm = (LDS_AS char*)smem;
  const unsigned sm0 = (unsigned)(size_t)sm;
  const int tid = threadIdx.x, lane = tid & 63;
  const int w = __builtin_amdgcn_readfirstlane(tid >> 6);
  const int wg = blockIdx.x;

  int tot = 0;
#pragma unroll
  for (int b = 0; b < B; ++b) tot += (eff_len(lens[b]) + TILE - 1) / TILE;
  const int T = tot * HKV;
  const int g0 = wg * T / NWG, g1 = (wg + 1) * T / NWG;
  if (g0 == g1) return;
  Cur cc; { int acc = 0, sb = 0, sP = 0, snt = 1, sl = 1;
#pragma unroll
    for (int b = 0; b < B; ++b) { const int l = eff_len(lens[b]), nt = (l + TILE - 1) / TILE;
      if (acc <= g0) { sb = b; sP = acc; snt = nt; sl = l; }
      acc += nt * HKV; }
    cc.b = sb; cc.h = (g0 - sP) / snt; cc.t = (g0 - sP) % snt; cc.nt = snt; cc.len = sl; cc.isq = 1; }
  int nst = g1 - g0; { Cur c = cc; int rem = g1 - g0; while (rem > 0) { const int n = min(rem, c.nt - c.t); rem -= n; ++nst;
      c.t = c.nt - 1; c.isq = 0; cur_advance(c, lens); } }

  if (w >= 4) {
    const int lw = w - 4;
    unsigned voff[4];
#pragma unroll
    for (int i = 0; i < 4; ++i) { const unsigned r = 4 * i + (lane >> 4); voff[i] = r * 2048u + (((unsigned)lane & 15u) ^ swz(r)) * 16u; }
    auto issue = [&](const Cur& c, int slot) {
      const unsigned ldsb = sm0 + (unsigned)slot * STAGE_BYTES;
      if (c.isq) {
        const char* qbase = (const char*)q + (size_t)((c.b * HQ + c.h * G) * SQ + 32 * lw) * (D * 2);
#pragma unroll
        for (int i = 0; i < 8; ++i) dma16<false>(qbase, (voff[i & 3] & 2047u) + (voff[i & 3] >> 11) * 256u + (i >> 2) * 4096u, ldsb + (32 * lw + 4 * i) * 256u);
      } else {
        const int phys = bt[c.b * NBPS + c.t * 4 + lw];
        const size_t poff = (size_t)phys * (PAGE * HKV * D * 2) + (size_t)c.h * (D * 2);
        const char* kb = (const char*)kc + poff; const char* vb = (const char*)vc + poff;
#pragma unroll
        for (int i = 0; i < 4; ++i) dma16<true>(kb, voff[i], ldsb + (16 * lw + 4 * i) * 256u);
#pragma unroll
        for (int i = 0; i < 4; ++i) dma16<true>(vb, voff[i], ldsb + 16384u + (16 * lw + 4 * i) * 256u);
      }
    };
    int issued = 0;
    for (; issued < NS - 1 && issued < nst; ++issued) { issue(cc, issued); cur_advance(cc, lens); }
    for (int i = 0; i < nst; ++i) {
      const int ahead = issued - i - 1;
      if (ahead >= 2) WAITBAR(16); else if (ahead == 1) WAITBAR(8); else WAITBAR(0);
      static_assert(NS == 4, "wait table");
      if (issued < nst) { issue(cc, issued % NS); cur_advance(cc, lens); ++issued; }
    }
    return;
  }

  const int qb = w, r32 = lane & 31, hi = lane >> 5;
  bf16x8 qf[8];
  f32x16 o[4];
#pragma unroll
  for (int d = 0; d < 4; ++d) o[d] = f32x16{};
  float m = -__builtin_inff(), l = 0.f;
  int fb = 0, fh = 0;

  auto flush = [&]() {
    const float lf = swap32_sum(l);
    const int slot = wg + fb * HKV + fh;
    float* dst = po + (size_t)slot * (128 * D) + (size_t)(qb * 4) * 1024 + lane * 4;
#pragma unroll
    for (int d = 0; d < 4; ++d)
#pragma unroll
      for (int g = 0; g < 4; ++g) *(f32x4*)(dst + (d * 4 + g) * 256) = f32x4{o[d][4 * g], o[d][4 * g + 1], o[d][4 * g + 2], o[d][4 * g + 3]};
    if (hi == 0) *(f32x2*)(pml + ((size_t)slot * 128 + qb * 32 + r32) * 2) = f32x2{m, lf};
#pragma unroll
    for (int d = 0; d < 4; ++d) o[d] = f32x16{};
    m = -__builtin_inff(); l = 0.f;
  };

  const unsigned kaddr0 = r32 * 256u + (((unsigned)hi) ^ swz(r32)) * 16u;
  const unsigned tq = (lane & 15) >> 2, tp = lane & 3, tblk = (lane >> 4) & 1;
  const unsigned vaddr0 = 16384u + (4 * hi + tq) * 256u + ((2 * tblk + (tp >> 1)) ^ ((tq << 2) | (unsigned)hi)) * 16u + 8u * (tp & 1);
  const unsigned qrow = 32 * qb + r32;
  const unsigned qaddr0 = qrow * 256u + (((unsigned)hi) ^ swz(qrow)) * 16u;

  bool first = true;
  for (int i = 0; i < nst; ++i) {
    asm volatile("s_barrier" ::: "memory");
    const unsigned sbase = (unsigned)(i % NS) * STAGE_BYTES;
    if (cc.isq) {
      if (!first) flush();
      first = false; fb = cc.b; fh = cc.h;
      const bool zq = lens[cc.b] <= 0;
      const unsigned qs = sbase + qaddr0;
#pragma unroll
      for (int s = 0; s < 8; ++s) { qf[s] = *(LDS_AS bf16x8*)(sm + (qs ^ (s << 5))); if (zq) qf[s] = bf16x8{}; }
    } else {
      const int nvalid = cc.len - cc.t * TILE;
      const unsigned kb0 = sbase + kaddr0;
      f32x16 s0 = f32x16{}, s1 = f32x16{};
#pragma unroll
      for (int ks = 0; ks < 8; ++ks) {
        const bf16x8 a0 = *(LDS_AS bf16x8*)(sm + (kb0 ^ (ks << 5)));
        const bf16x8 a1 = *(LDS_AS bf16x8*)(sm + (kb0 ^ (ks << 5)) + 8192);
        s0 = __builtin_amdgcn_mfma_f32_32x32x16_bf16(a0, qf[ks], s0, 0, 0, 0);
        s1 = __builtin_amdgcn_mfma_f32_32x32x16_bf16(a1, qf[ks], s1, 0, 0, 0);
      }
      if (nvalid < TILE) {
        const int nvh = nvalid - 4 * hi;
#pragma unroll
        for (int r = 0; r < 16; ++r) { s0[r] = (r & 3) + 8 * (r >> 2) < nvh ? s0[r] : -__builtin_inff(); s1[r] = 32 + (r & 3) + 8 * (r >> 2) < nvh ? s1[r] : -__builtin_inff(); }
      }
      float tmax = fmaxf(s0[0], s1[0]);
#pragma unroll
      for (int r = 1; r < 16; ++r) tmax = fmaxf(tmax, fmaxf(s0[r], s1[r]));
      tmax = swap32_max(tmax) * SCALE_LOG2E;
      if (__any(tmax > m + 8.f)) {
        const float mn = fmaxf(m, tmax), alpha = __builtin_amdgcn_exp2f(m - mn);
        m = mn; l *= alpha;
#pragma unroll
        for (int d = 0; d < 4; ++d)
#pragma unroll
          for (int r = 0; r < 16; ++r) o[d][r] *= alpha;
      }
      float ps = 0.f;
#pragma unroll
      for (int r = 0; r < 16; ++r) { s0[r] = __builtin_amdgcn_exp2f(fmaf(s0[r], SCALE_LOG2E, -m)); ps += s0[r]; }
#pragma unroll
      for (int r = 0; r < 16; ++r) { s1[r] = __builtin_amdgcn_exp2f(fmaf(s1[r], SCALE_LOG2E, -m)); ps += s1[r]; }
      l += ps;
      bf16x8 pb[4];
#pragma unroll
      for (int ks = 0; ks < 2; ++ks) {
        u32x4 u = {pack2(s0[8 * ks], s0[8 * ks + 1]), pack2(s0[8 * ks + 2], s0[8 * ks + 3]), pack2(s0[8 * ks + 4], s0[8 * ks + 5]), pack2(s0[8 * ks + 6], s0[8 * ks + 7])};
        pb[ks] = __builtin_bit_cast(bf16x8, u);
        u32x4 v = {pack2(s1[8 * ks], s1[8 * ks + 1]), pack2(s1[8 * ks + 2], s1[8 * ks + 3]), pack2(s1[8 * ks + 4], s1[8 * ks + 5]), pack2(s1[8 * ks + 6], s1[8 * ks + 7])};
        pb[2 + ks] = __builtin_bit_cast(bf16x8, v);
      }
      const unsigned vb0 = sbase + vaddr0;
#pragma unroll
      for (int ks = 0; ks < 4; ++ks)
#pragma unroll
        for (int d = 0; d < 4; ++d) {
          const s16x4 lo = __builtin_amdgcn_ds_read_tr16_b64_v4i16((LDS_AS s16x4*)(sm + (vb0 ^ (d << 6)) + ks * 4096));
          const s16x4 up = __builtin_amdgcn_ds_read_tr16_b64_v4i16((LDS_AS s16x4*)(sm + (vb0 ^ ((d << 6) | 32)) + ks * 4096 + 2048));
          const bf16x8 a = __builtin_shufflevector(lo, up, 0, 1, 2, 3, 4, 5, 6, 7);
          o[d] = __builtin_amdgcn_mfma_f32_32x32x16_bf16(a, pb[ks], o[d], 0, 0, 0);
        }
    }
    cur_advance(cc, lens);
  }
  flush();
}

__global__ __launch_bounds__(256) void attn_combine(const int* __restrict__ lens, const float* __restrict__ po, const float* __restrict__ pml,
                                                    __hip_bfloat16* __restrict__ out) {
  const int p = blockIdx.x >> 2, qb = blockIdx.x & 3, d0 = threadIdx.x >> 6, lane = threadIdx.x & 63, r32 = lane & 31, hi = lane >> 5;
  const int b = p >> 3, h = p & 7;
  int tot = 0, P = 0, ntb = 1;
#pragma unroll
  for (int bb = 0; bb < B; ++bb) { const int nt = (eff_len(lens[bb]) + TILE - 1) / TILE; if (bb == b) { P = tot * HKV + h * nt; ntb = nt; } tot += nt; }
  const int T = tot * HKV;
  const int i0 = ((P + 1) * NWG + T - 1) / T - 1, i1 = ((P + ntb) * NWG + T - 1) / T - 1;
  float M = -__builtin_inff();
  for (int i = i0; i <= i1; ++i) { if (i * T / NWG == (i + 1) * T / NWG) continue;
    M = fmaxf(M, pml[((size_t)(i + p) * 128 + qb * 32 + r32) * 2]); }
  float L = 0.f; f32x4 acc[4] = {};
  for (int i = i0; i <= i1; ++i) { if (i * T / NWG == (i + 1) * T / NWG) continue;
    const f32x2 ml = *(const f32x2*)(pml + ((size_t)(i + p) * 128 + qb * 32 + r32) * 2);
    const float f = __builtin_amdgcn_exp2f(ml[0] - M); L += ml[1] * f;
    const float* src = po + (size_t)(i + p) * (128 * D) + (size_t)(qb * 4 + d0) * 1024 + lane * 4;
#pragma unroll
    for (int g = 0; g < 4; ++g) { const f32x4 x = *(const f32x4*)(src + g * 256); acc[g] += x * f; }
  }
  const float inv = 1.f / L;
  __hip_bfloat16* orow = out + ((size_t)((b * HQ + h * G) * SQ) + qb * 32 + r32) * D + 32 * d0 + 4 * hi;
#pragma unroll
  for (int g = 0; g < 4; ++g) { u32x2 u = {pack2(acc[g][0] * inv, acc[g][1] * inv), pack2(acc[g][2] * inv, acc[g][3] * inv)};
    *(u32x2*)(orow + 8 * g) = u; }
}

}

extern "C" void kernel_launch(void* const* d_in, const int* in_sizes, int n_in, void* d_out, int out_size, void* d_ws, size_t ws_size,
                              hipStream_t stream) {
  const __hip_bfloat16* q = (const __hip_bfloat16*)d_in[0];
  const __hip_bfloat16* kc = (const __hip_bfloat16*)d_in[1];
  const __hip_bfloat16* vc = (const __hip_bfloat16*)d_in[2];
  const int* bt = (const int*)d_in[3];
  const int* lens = (const int*)d_in[4];
  float* pml = (float*)d_ws;
  float* po = (float*)((char*)d_ws + (1u << 20));
  attn_split<<<NWG, 512, 0, stream>>>(q, kc, vc, bt, lens, po, pml);
  attn_combine<<<B * HKV * 4, 256, 0, stream>>>(lens, po, pml, (__hip_bfloat16*)d_out);
}
```

```cpp
#include <hip/hip_runtime.h>
#include <hip/hip_bf16.h>
#include <stdint.h>

namespace {

constexpr int B = 16, HQ = 32, HKV = 8, G = HQ / HKV, SQ = 32, D = 128, PAGE = 16, NBPS = 256, SKV = NBPS * PAGE;
constexpr int TILE = 64;
constexpr int NWG = 256;
constexpr int NS = 4;
constexpr int STAGE_BYTES = 32768;
constexpr int SCR_OFF = NS * STAGE_BYTES;
constexpr int SCR_ML = SCR_OFF + 16384;
constexpr int LDS_BYTES = SCR_ML + 1024;
constexpr float SCALE_LOG2E = 0.08838834764831845f * 1.4426950408889634f;

typedef short bf16x8 __attribute__((ext_vector_type(8)));
typedef short s16x4 __attribute__((ext_vector_type(4)));
typedef float f32x16 __attribute__((ext_vector_type(16)));
typedef float f32x4 __attribute__((ext_vector_type(4)));
typedef float f32x2 __attribute__((ext_vector_type(2)));
typedef __bf16 bfx2 __attribute__((ext_vector_type(2)));
typedef unsigned u32x4 __attribute__((ext_vector_type(4)));
typedef unsigned u32x2 __attribute__((ext_vector_type(2)));
#define LDS_AS __attribute__((address_space(3)))

__device__ __forceinline__ int eff_len(int l) { l = l > SKV ? SKV : l; return l <= 0 ? SKV : l; }
__device__ __forceinline__ unsigned swz(unsigned row) { return ((row & 3u) << 2) | ((row >> 2) & 3u); }
__device__ __forceinline__ float swap32_max(float x) {
  auto r = __builtin_amdgcn_permlane32_swap(__float_as_uint(x), __float_as_uint(x), false, false);
  return fmaxf(__uint_as_float(r[0]), __uint_as_float(r[1]));
}
__device__ __forceinline__ float swap32_sum(float x) {
  auto r = __builtin_amdgcn_permlane32_swap(__float_as_uint(x), __float_as_uint(x), false, false);
  return __uint_as_float(r[0]) + __uint_as_float(r[1]);
}
__device__ __forceinline__ unsigned pack2(float a, float b) {
  f32x2 f = {a, b};
  return __builtin_bit_cast(unsigned, __builtin_convertvector(f, bfx2));
}

template <bool NT>
__device__ __forceinline__ void dma16(const void* sbase, unsigned voff, unsigned lds_dst) {
  unsigned keep;
  if (NT)
    asm volatile("s_nop 4\n\ts_mov_b32 %0, m0\n\ts_mov_b32 m0, %3\n\ts_nop 0\n\tglobal_load_lds_dwordx4 %1, %2 nt\n\ts_mov_b32 m0, %0"
                 : "=&s"(keep) : "v"(voff), "s"(sbase), "s"(lds_dst) : "memory");
  else
    asm volatile("s_nop 4\n\ts_mov_b32 %0, m0\n\ts_mov_b32 m0, %3\n\ts_nop 0\n\tglobal_load_lds_dwordx4 %1, %2\n\ts_mov_b32 m0, %0"
                 : "=&s"(keep) : "v"(voff), "s"(sbase), "s"(lds_dst) : "memory");
}

struct Cur { int b, h, t, nt, len, isq; };

__device__ __forceinline__ void cur_advance(Cur& c, const int* lens) {
  if (c.isq) { c.isq = 0; return; }
  if (++c.t == c.nt) {
    c.t = 0; c.isq = 1;
    if (++c.h == HKV) {
      c.h = 0; ++c.b;
      const int bb = c.b < B ? c.b : B - 1;
      c.len = eff_len(lens[bb]); c.nt = (c.len + TILE - 1) / TILE;
    }
  }
}

#define WAITBAR(n) asm volatile("s_waitcnt vmcnt(" #n ")\n\ts_barrier" ::: "memory")
#define LBAR() asm volatile("s_waitcnt lgkmcnt(0)\n\ts_barrier" ::: "memory")

__global__ __launch_bounds__(512, 2) void attn_split(const __hip_bfloat16* __restrict__ q, const __hip_bfloat16* __restrict__ kc,
                                                     const __hip_bfloat16* __restrict__ vc, const int* __restrict__ bt,
                                                     const int* __restrict__ lens, float* __restrict__ po, float* __restrict__ pml) {
  __shared__ __attribute__((aligned(256))) char smem[NS * STAGE_BYTES];
  LDS_AS char* const sm = (LDS_AS char*)smem;
  const unsigned sm0 = (unsigned)(size_t)sm;
  const int tid = threadIdx.x, lane = tid & 63;
  const int w = __builtin_amdgcn_readfirstlane(tid >> 6);
  const int wg = blockIdx.x;

  int tot = 0;
#pragma unroll
  for (int b = 0; b < B; ++b) tot += (eff_len(lens[b]) + TILE - 1) / TILE;
  const int T = tot * HKV;
  const int g0 = wg * T / NWG, g1 = (wg + 1) * T / NWG;
  if (g0 == g1) return;
  Cur cc; { int acc = 0, sb = 0, sP = 0, snt = 1, sl = 1;
#pragma unroll
    for (int b = 0; b < B; ++b) { const int l = eff_len(lens[b]), nt = (l + TILE - 1) / TILE;
      if (acc <= g0) { sb = b; sP = acc; snt = nt; sl = l; }
      acc += nt * HKV; }
    cc.b = sb; cc.h = (g0 - sP) / snt; cc.t = (g0 - sP) % snt; cc.nt = snt; cc.len = sl; cc.isq = 1; }
  int nst = g1 - g0; { Cur c = cc; int rem = g1 - g0; while (rem > 0) { const int n = min(rem, c.nt - c.t); rem -= n; ++nst;
      c.t = c.nt - 1; c.isq = 0; cur_advance(c, lens); } }

  if (w >= 4) {
    const int lw = w - 4;
    unsigned voff[4];
#pragma unroll
    for (int i = 0; i < 4; ++i) { const unsigned r = 4 * i + (lane >> 4); voff[i] = r * 2048u + (((unsigned)lane & 15u) ^ swz(r)) * 16u; }
    auto issue = [&](const Cur& c, int slot) {
      const unsigned ldsb = sm0 + (unsigned)slot * STAGE_BYTES;
      if (c.isq) {
        const char* qbase = (const char*)q + (size_t)((c.b * HQ + c.h * G) * SQ + 32 * lw) * (D * 2);
#pragma unroll
        for (int i = 0; i < 8; ++i) dma16<false>(qbase, (voff[i & 3] & 2047u) + (voff[i & 3] >> 11) * 256u + (i >> 2) * 4096u, ldsb + (32 * lw + 4 * i) * 256u);
      } else {
        const int phys = bt[c.b * NBPS + c.t * 4 + lw];
        const size_t poff = (size_t)phys * (PAGE * HKV * D * 2) + (size_t)c.h * (D * 2);
        const char* kb = (const char*)kc + poff; const char* vb = (const char*)vc + poff;
#pragma unroll
        for (int i = 0; i < 4; ++i) dma16<true>(kb, voff[i], ldsb + (16 * lw + 4 * i) * 256u);
#pragma unroll
        for (int i = 0; i < 4; ++i) dma16<true>(vb, voff[i], ldsb + 16384u + (16 * lw + 4 * i) * 256u);
      }
    };
    int issued = 0;
    for (; issued < NS - 1 && issued < nst; ++issued) { issue(cc, issued); cur_advance(cc, lens); }
    for (int i = 0; i < nst; ++i) {
      const int ahead = issued - i - 1;
      if (ahead >= 2) WAITBAR(16); else if (ahead == 1) WAITBAR(8); else WAITBAR(0);
      static_assert(NS == 4, "wait table");
      if (issued < nst) { issue(cc, issued % NS); cur_advance(cc, lens); ++issued; }
    }
    return;
  }

  const int qb = w, r32 = lane & 31, hi = lane >> 5;
  bf16x8 qf[8];
  f32x16 o[4];
#pragma unroll
  for (int d = 0; d < 4; ++d) o[d] = f32x16{};
  float m = -__builtin_inff(), l = 0.f;
  int fb = 0, fh = 0;

  auto flush = [&]() {
    const float lf = swap32_sum(l);
    const int slot = wg + fb * HKV + fh;
    float* dst = po + (size_t)slot * (128 * D) + (size_t)(qb * 4) * 1024 + lane * 4;
#pragma unroll
    for (int d = 0; d < 4; ++d)
#pragma unroll
      for (int g = 0; g < 4; ++g) *(f32x4*)(dst + (d * 4 + g) * 256) = f32x4{o[d][4 * g], o[d][4 * g + 1], o[d][4 * g + 2], o[d][4 * g + 3]};
    if (hi == 0) *(f32x2*)(pml + ((size_t)slot * 128 + qb * 32 + r32) * 2) = f32x2{m, lf};
#pragma unroll
    for (int d = 0; d < 4; ++d) o[d] = f32x16{};
    m = -__builtin_inff(); l = 0.f;
  };

  const unsigned kaddr0 = r32 * 256u + (((unsigned)hi) ^ swz(r32)) * 16u;
  const unsigned tq = (lane & 15) >> 2, tp = lane & 3, tblk = (lane >> 4) & 1;
  const unsigned vaddr0 = 16384u + (4 * hi + tq) * 256u + ((2 * tblk + (tp >> 1)) ^ ((tq << 2) | (unsigned)hi)) * 16u + 8u * (tp & 1);
  const unsigned qrow = 32 * qb + r32;
  const unsigned qaddr0 = qrow * 256u + (((unsigned)hi) ^ swz(qrow)) * 16u;

  int i = 0;
  while (i < nst) {
    asm volatile("s_barrier" ::: "memory");
    fb = cc.b; fh = cc.h;
    { const bool zq = lens[cc.b] <= 0;
      const unsigned qs = (unsigned)(i % NS) * STAGE_BYTES + qaddr0;
#pragma unroll
      for (int s = 0; s < 8; ++s) { qf[s] = *(LDS_AS bf16x8*)(sm + (qs ^ (s << 5))); if (zq) qf[s] = bf16x8{}; } }
    ++i;
    const int ntile = min(nst - i, cc.nt - cc.t);
    int nleft = cc.len - cc.t * TILE;
    for (int tt = 0; tt < ntile; ++tt, ++i, nleft -= TILE) {
      asm volatile("s_barrier" ::: "memory");
      const unsigned sbase = (unsigned)(i % NS) * STAGE_BYTES;
      const unsigned kb0 = sm0 + sbase + kaddr0;
      bf16x8 ka[8], kb_[8];
#define KLD(s) asm volatile("ds_read_b128 %0, %2\n\tds_read_b128 %1, %2 offset:8192" : "=&v"(ka[s]), "=&v"(kb_[s]) : "v"(kb0 ^ ((s) << 5)) : "memory")
      KLD(0); KLD(1); KLD(2); KLD(3); KLD(4); KLD(5); KLD(6); KLD(7);
#undef KLD
      f32x16 s0 = f32x16{}, s1 = f32x16{};
#define KW(n, s) asm volatile("s_waitcnt lgkmcnt(" #n ")" : "+v"(ka[s]), "+v"(kb_[s]) :: "memory")
#define QK(s) s0 = __builtin_amdgcn_mfma_f32_32x32x16_bf16(ka[s], qf[s], s0, 0, 0, 0); s1 = __builtin_amdgcn_mfma_f32_32x32x16_bf16(kb_[s], qf[s], s1, 0, 0, 0)
      KW(14, 0); QK(0); KW(12, 1); QK(1); KW(10, 2); QK(2); KW(8, 3); QK(3); KW(6, 4); QK(4); KW(4, 5); QK(5); KW(2, 6); QK(6); KW(0, 7); QK(7);
#undef KW
#undef QK
      if (nleft < TILE) {
        const int nvh = nleft - 4 * hi;
#pragma unroll
        for (int r = 0; r < 16; ++r) { s0[r] = (r & 3) + 8 * (r >> 2) < nvh ? s0[r] : -__builtin_inff(); s1[r] = 32 + (r & 3) + 8 * (r >> 2) < nvh ? s1[r] : -__builtin_inff(); }
      }
      float tmax = s0[0];
#pragma unroll
      for (int r = 1; r < 16; ++r) tmax = fmaxf(tmax, s0[r]);
#pragma unroll
      for (int r = 0; r < 16; ++r) tmax = fmaxf(tmax, s1[r]);
      tmax = swap32_max(tmax) * SCALE_LOG2E;
      if (__any(tmax > m + 8.f)) {
        const float mn = fmaxf(m, tmax), alpha = __builtin_amdgcn_exp2f(m - mn);
        m = mn; l *= alpha;
#pragma unroll
        for (int d = 0; d < 4; ++d)
#pragma unroll
          for (int r = 0; r < 16; ++r) o[d][r] *= alpha;
      }
      float ps = 0.f;
#pragma unroll
      for (int r = 0; r < 16; ++r) { s0[r] = __builtin_amdgcn_exp2f(fmaf(s0[r], SCALE_LOG2E, -m)); ps += s0[r]; }
#pragma unroll
      for (int r = 0; r < 16; ++r) { s1[r] = __builtin_amdgcn_exp2f(fmaf(s1[r], SCALE_LOG2E, -m)); ps += s1[r]; }
      l += ps;
      bf16x8 pb[4];
#pragma unroll
      for (int ks = 0; ks < 2; ++ks) {
        u32x4 u = {pack2(s0[8 * ks], s0[8 * ks + 1]), pack2(s0[8 * ks + 2], s0[8 * ks + 3]), pack2(s0[8 * ks + 4], s0[8 * ks + 5]), pack2(s0[8 * ks + 6], s0[8 * ks + 7])};
        pb[ks] = __builtin_bit_cast(bf16x8, u);
        u32x4 v = {pack2(s1[8 * ks], s1[8 * ks + 1]), pack2(s1[8 * ks + 2], s1[8 * ks + 3]), pack2(s1[8 * ks + 4], s1[8 * ks + 5]), pack2(s1[8 * ks + 6], s1[8 * ks + 7])};
        pb[2 + ks] = __builtin_bit_cast(bf16x8, v);
      }
      const unsigned vb0 = sbase + vaddr0;
#pragma unroll
      for (int ks = 0; ks < 4; ++ks)
#pragma unroll
        for (int d = 0; d < 4; ++d) {
          const s16x4 lo = __builtin_amdgcn_ds_read_tr16_b64_v4i16((LDS_AS s16x4*)(sm + (vb0 ^ (d << 6)) + ks * 4096));
          const s16x4 up = __builtin_amdgcn_ds_read_tr16_b64_v4i16((LDS_AS s16x4*)(sm + (vb0 ^ ((d << 6) | 32)) + ks * 4096 + 2048));
          const bf16x8 a = __builtin_shufflevector(lo, up, 0, 1, 2, 3, 4, 5, 6, 7);
          o[d] = __builtin_amdgcn_mfma_f32_32x32x16_bf16(a, pb[ks], o[d], 0, 0, 0);
        }
    }
    flush();
    cc.t = cc.nt - 1; cc.isq = 0; cur_advance(cc, lens);
  }
}

__global__ __launch_bounds__(256) void attn_combine(const int* __restrict__ lens, const float* __restrict__ po, const float* __restrict__ pml,
                                                    __hip_bfloat16* __restrict__ out) {
  const int p = blockIdx.x >> 2, qb = blockIdx.x & 3, d0 = threadIdx.x >> 6, lane = threadIdx.x & 63, r32 = lane & 31, hi = lane >> 5;
  const int b = p >> 3, h = p & 7;
  int tot = 0, P = 0, ntb = 1;
#pragma unroll
  for (int bb = 0; bb < B; ++bb) { const int nt = (eff_len(lens[bb]) + TILE - 1) / TILE; if (bb == b) { P = tot * HKV + h * nt; ntb = nt; } tot += nt; }
  const int T = tot * HKV;
  const int i0 = ((P + 1) * NWG + T - 1) / T - 1, i1 = ((P + ntb) * NWG + T - 1) / T - 1;
  float M = -__builtin_inff();
  for (int i = i0; i <= i1; ++i) { if (i * T / NWG == (i + 1) * T / NWG) continue;
    M = fmaxf(M, pml[((size_t)(i + p) * 128 + qb * 32 + r32) * 2]); }
  float L = 0.f; f32x4 acc[4] = {};
  for (int i = i0; i <= i1; ++i) { if (i * T / NWG == (i + 1) * T / NWG) continue;
    const f32x2 ml = *(const f32x2*)(pml + ((size_t)(i + p) * 128 + qb * 32 + r32) * 2);
    const float f = __builtin_amdgcn_exp2f(ml[0] - M); L += ml[1] * f;
    const float* src = po + (size_t)(i + p) * (128 * D) + (size_t)(qb * 4 + d0) * 1024 + lane * 4;
#pragma unroll
    for (int g = 0; g < 4; ++g) { const f32x4 x = *(const f32x4*)(src + g * 256); acc[g] += x * f; }
  }
  const float inv = 1.f / L;
  __hip_bfloat16* orow = out + ((size_t)((b * HQ + h * G) * SQ) + qb * 32 + r32) * D + 32 * d0 + 4 * hi;
#pragma unroll
  for (int g = 0; g < 4; ++g) { u32x2 u = {pack2(acc[g][0] * inv, acc[g][1] * inv), pack2(acc[g][2] * inv, acc[g][3] * inv)};
    *(u32x2*)(orow + 8 * g) = u; }
}

}

extern "C" void kernel_launch(void* const* d_in, const int* in_sizes, int n_in, void* d_out, int out_size, void* d_ws, size_t ws_size,
                              hipStream_t stream) {
  const __hip_bfloat16* q = (const __hip_bfloat16*)d_in[0];
  const __hip_bfloat16* kc = (const __hip_bfloat16*)d_in[1];
  const __hip_bfloat16* vc = (const __hip_bfloat16*)d_in[2];
  const int* bt = (const int*)d_in[3];
  const int* lens = (const int*)d_in[4];
  float* pml = (float*)d_ws;
  float* po = (float*)((char*)d_ws + (1u << 20));
  attn_split<<<NWG, 512, 0, stream>>>(q, kc, vc, bt, lens, po, pml);
  attn_combine<<<B * HKV * 4, 256, 0, stream>>>(lens, po, pml, (__hip_bfloat16*)d_out);
}
```

```cpp
#include <hip/hip_runtime.h>
#include <hip/hip_bf16.h>
#include <stdint.h>

namespace {

constexpr int B = 16, HQ = 32, HKV = 8, G = HQ / HKV, SQ = 32, D = 128, PAGE = 16, NBPS = 256, SKV = NBPS * PAGE;
constexpr int TILE = 64;
constexpr int NWG = 256;
constexpr int NS = 4;
constexpr int STAGE_BYTES = 32768;
constexpr float SCALE_LOG2E = 0.08838834764831845f * 1.4426950408889634f;

typedef short bf16x8 __attribute__((ext_vector_type(8)));
typedef short s16x4 __attribute__((ext_vector_type(4)));
typedef float f32x16 __attribute__((ext_vector_type(16)));
typedef float f32x4 __attribute__((ext_vector_type(4)));
typedef float f32x2 __attribute__((ext_vector_type(2)));
typedef __bf16 bfx2 __attribute__((ext_vector_type(2)));
typedef unsigned u32x4 __attribute__((ext_vector_type(4)));
typedef unsigned u32x2 __attribute__((ext_vector_type(2)));
#define LDS_AS __attribute__((address_space(3)))

__device__ __forceinline__ int eff_len(int l) { l = l > SKV ? SKV : l; return l <= 0 ? SKV : l; }
__device__ __forceinline__ unsigned swz(unsigned row) { return ((row & 3u) << 2) | ((row >> 2) & 3u); }
__device__ __forceinline__ float swap32_max(float x) {
  auto r = __builtin_amdgcn_permlane32_swap(__float_as_uint(x), __float_as_uint(x), false, false);
  return fmaxf(__uint_as_float(r[0]), __uint_as_float(r[1]));
}
__device__ __forceinline__ float swap32_sum(float x) {
  auto r = __builtin_amdgcn_permlane32_swap(__float_as_uint(x), __float_as_uint(x), false, false);
  return __uint_as_float(r[0]) + __uint_as_float(r[1]);
}
__device__ __forceinline__ unsigned pack2(float a, float b) {
  f32x2 f = {a, b};
  return __builtin_bit_cast(unsigned, __builtin_convertvector(f, bfx2));
}

template <bool NT>
__device__ __forceinline__ void dma16(const void* sbase, unsigned voff, unsigned lds_dst) {
  unsigned keep;
  if (NT)
    asm volatile("s_nop 4\n\ts_mov_b32 %0, m0\n\ts_mov_b32 m0, %3\n\ts_nop 0\n\tglobal_load_lds_dwordx4 %1, %2 nt\n\ts_mov_b32 m0, %0"
                 : "=&s"(keep) : "v"(voff), "s"(sbase), "s"(lds_dst) : "memory");
  else
    asm volatile("s_nop 4\n\ts_mov_b32 %0, m0\n\ts_mov_b32 m0, %3\n\ts_nop 0\n\tglobal_load_lds_dwordx4 %1, %2\n\ts_mov_b32 m0, %0"
                 : "=&s"(keep) : "v"(voff), "s"(sbase), "s"(lds_dst) : "memory");
}

struct Cur { int b, h, t, nt, len, isq; };

__device__ __forceinline__ void cur_advance(Cur& c, const int* lens) {
  if (c.isq) { c.isq = 0; return; }
  if (++c.t == c.nt) {
    c.t = 0; c.isq = 1;
    if (++c.h == HKV) {
      c.h = 0; ++c.b;
      const int bb = c.b < B ? c.b : B - 1;
      c.len = eff_len(lens[bb]); c.nt = (c.len + TILE - 1) / TILE;
    }
  }
}

#define WAITBAR(n) asm volatile("s_waitcnt vmcnt(" #n ")\n\ts_barrier" ::: "memory")
#define LBAR() asm volatile("s_waitcnt lgkmcnt(0)\n\ts_barrier" ::: "memory")

__global__ __launch_bounds__(512, 2) void attn_split(const __hip_bfloat16* __restrict__ q, const __hip_bfloat16* __restrict__ kc,
                                                     const __hip_bfloat16* __restrict__ vc, const int* __restrict__ bt,
                                                     const int* __restrict__ lens, float* __restrict__ po, float* __restrict__ pml) {
  __shared__ __attribute__((aligned(256))) char smem[NS * STAGE_BYTES];
  LDS_AS char* const sm = (LDS_AS char*)smem;
  const unsigned sm0 = (unsigned)(size_t)sm;
  const int tid = threadIdx.x, lane = tid & 63;
  const int w = __builtin_amdgcn_readfirstlane(tid >> 6);
  const int wg = blockIdx.x;

  int tot = 0;
#pragma unroll
  for (int b = 0; b < B; ++b) tot += (eff_len(lens[b]) + TILE - 1) / TILE;
  const int T = tot * HKV;
  const int g0 = wg * T / NWG, g1 = (wg + 1) * T / NWG;
  if (g0 == g1) return;
  Cur cc; { int acc = 0, sb = 0, sP = 0, snt = 1, sl = 1;
#pragma unroll
    for (int b = 0; b < B; ++b) { const int l = eff_len(lens[b]), nt = (l + TILE - 1) / TILE;
      if (acc <= g0) { sb = b; sP = acc; snt = nt; sl = l; }
      acc += nt * HKV; }
    cc.b = sb; cc.h = (g0 - sP) / snt; cc.t = (g0 - sP) % snt; cc.nt = snt; cc.len = sl; cc.isq = 1; }
  int nst = g1 - g0; { Cur c = cc; int rem = g1 - g0; while (rem > 0) { const int n = min(rem, c.nt - c.t); rem -= n; ++nst;
      c.t = c.nt - 1; c.isq = 0; cur_advance(c, lens); } }

  if (w >= 4) {
    const int lw = w - 4;
    unsigned voff[4];
#pragma unroll
    for (int i = 0; i < 4; ++i) { const unsigned r = 4 * i + (lane >> 4); voff[i] = r * 2048u + (((unsigned)lane & 15u) ^ swz(r)) * 16u; }
    auto issue = [&](const Cur& c, int slot) {
      const unsigned ldsb = sm0 + (unsigned)slot * STAGE_BYTES;
      if (c.isq) {
        const char* qbase = (const char*)q + (size_t)((c.b * HQ + c.h * G) * SQ + 32 * lw) * (D * 2);
#pragma unroll
        for (int i = 0; i < 8; ++i) dma16<false>(qbase, (voff[i & 3] & 2047u) + (voff[i & 3] >> 11) * 256u + (i >> 2) * 4096u, ldsb + (32 * lw + 4 * i) * 256u);
      } else {
        const int phys = bt[c.b * NBPS + c.t * 4 + lw];
        const size_t poff = (size_t)phys * (PAGE * HKV * D * 2) + (size_t)c.h * (D * 2);
        const char* kb = (const char*)kc + poff; const char* vb = (const char*)vc + poff;
#pragma unroll
        for (int i = 0; i < 4; ++i) dma16<true>(kb, voff[i], ldsb + (16 * lw + 4 * i) * 256u);
#pragma unroll
        for (int i = 0; i < 4; ++i) dma16<true>(vb, voff[i], ldsb + 16384u + (16 * lw + 4 * i) * 256u);
      }
    };
    int issued = 0;
    for (; issued < NS - 1 && issued < nst; ++issued) { issue(cc, issued); cur_advance(cc, lens); }
    for (int i = 0; i < nst; ++i) {
      const int ahead = issued - i - 1;
      if (ahead >= 2) WAITBAR(16); else if (ahead == 1) WAITBAR(8); else WAITBAR(0);
      static_assert(NS == 4, "wait table");
      if (issued < nst) { issue(cc, issued % NS); cur_advance(cc, lens); ++issued; }
    }
    return;
  }

  const int qb = w, r32 = lane & 31, hi = lane >> 5;
  bf16x8 qf[8];
  f32x16 o[4];
#pragma unroll
  for (int d = 0; d < 4; ++d) o[d] = f32x16{};
  float m = -__builtin_inff(), l = 0.f;
  int fb = 0, fh = 0;

  auto flush = [&]() {
    const float lf = swap32_sum(l);
    const int slot = wg + fb * HKV + fh;
    unsigned* dst = (unsigned*)po + (size_t)slot * (64 * D) + (size_t)(qb * 4) * 512 + lane * 2;
    const float inv = 1.f / lf;
#pragma unroll
    for (int d = 0; d < 4; ++d)
#pragma unroll
      for (int g = 0; g < 4; ++g) *(u32x2*)(dst + (d * 4 + g) * 128) = u32x2{pack2(o[d][4 * g] * inv, o[d][4 * g + 1] * inv), pack2(o[d][4 * g + 2] * inv, o[d][4 * g + 3] * inv)};
    if (hi == 0) *(f32x2*)(pml + ((size_t)slot * 128 + qb * 32 + r32) * 2) = f32x2{m, lf};
#pragma unroll
    for (int d = 0; d < 4; ++d) o[d] = f32x16{};
    m = -__builtin_inff(); l = 0.f;
  };

  const unsigned kaddr0 = r32 * 256u + (((unsigned)hi) ^ swz(r32)) * 16u;
  const unsigned tq = (lane & 15) >> 2, tp = lane & 3, tblk = (lane >> 4) & 1;
  const unsigned vaddr0 = 16384u + (4 * hi + tq) * 256u + ((2 * tblk + (tp >> 1)) ^ ((tq << 2) | (unsigned)hi)) * 16u + 8u * (tp & 1);
  const unsigned qrow = 32 * qb + r32;
  const unsigned qaddr0 = qrow * 256u + (((unsigned)hi) ^ swz(qrow)) * 16u;

  int i = 0;
  while (i < nst) {
    asm volatile("s_barrier" ::: "memory");
    fb = cc.b; fh = cc.h;
    { const bool zq = lens[cc.b] <= 0;
      const unsigned qs = (unsigned)(i % NS) * STAGE_BYTES + qaddr0;
#pragma unroll
      for (int s = 0; s < 8; ++s) { qf[s] = *(LDS_AS bf16x8*)(sm + (qs ^ (s << 5))); if (zq) qf[s] = bf16x8{}; } }
    ++i;
    const int ntile = min(nst - i, cc.nt - cc.t);
    int nleft = cc.len - cc.t * TILE;
    for (int tt = 0; tt < ntile; ++tt, ++i, nleft -= TILE) {
      asm volatile("s_barrier" ::: "memory");
      const unsigned sbase = (unsigned)(i % NS) * STAGE_BYTES;
      const unsigned kb0 = sm0 + sbase + kaddr0;
      bf16x8 ka[8], kb_[8];
#define KLD(s) asm volatile("ds_read_b128 %0, %2\n\tds_read_b128 %1, %2 offset:8192" : "=&v"(ka[s]), "=&v"(kb_[s]) : "v"(kb0 ^ ((s) << 5)) : "memory")
      KLD(0); KLD(1); KLD(2); KLD(3); KLD(4); KLD(5); KLD(6); KLD(7);
#undef KLD
      f32x16 s0 = f32x16{}, s1 = f32x16{};
#define KW(n, s) asm volatile("s_waitcnt lgkmcnt(" #n ")" : "+v"(ka[s]), "+v"(kb_[s]) :: "memory")
#define QK(s) s0 = __builtin_amdgcn_mfma_f32_32x32x16_bf16(ka[s], qf[s], s0, 0, 0, 0); s1 = __builtin_amdgcn_mfma_f32_32x32x16_bf16(kb_[s], qf[s], s1, 0, 0, 0)
      KW(14, 0); QK(0); KW(12, 1); QK(1); KW(10, 2); QK(2); KW(8, 3); QK(3); KW(6, 4); QK(4); KW(4, 5); QK(5); KW(2, 6); QK(6); KW(0, 7); QK(7);
#undef KW
#undef QK
      if (nleft < TILE) {
        const int nvh = nleft - 4 * hi;
#pragma unroll
        for (int r = 0; r < 16; ++r) { s0[r] = (r & 3) + 8 * (r >> 2) < nvh ? s0[r] : -__builtin_inff(); s1[r] = 32 + (r & 3) + 8 * (r >> 2) < nvh ? s1[r] : -__builtin_inff(); }
      }
      float tmax = s0[0];
#pragma unroll
      for (int r = 1; r < 16; ++r) tmax = fmaxf(tmax, s0[r]);
#pragma unroll
      for (int r = 0; r < 16; ++r) tmax = fmaxf(tmax, s1[r]);
      tmax = swap32_max(tmax) * SCALE_LOG2E;
      if (__any(tmax > m + 8.f)) {
        const float mn = fmaxf(m, tmax), alpha = __builtin_amdgcn_exp2f(m - mn);
        m = mn; l *= alpha;
#pragma unroll
        for (int d = 0; d < 4; ++d)
#pragma unroll
          for (int r = 0; r < 16; ++r) o[d][r] *= alpha;
      }
      float ps = 0.f;
#pragma unroll
      for (int r = 0; r < 16; ++r) { s0[r] = __builtin_amdgcn_exp2f(fmaf(s0[r], SCALE_LOG2E, -m)); ps += s0[r]; }
#pragma unroll
      for (int r = 0; r < 16; ++r) { s1[r] = __builtin_amdgcn_exp2f(fmaf(s1[r], SCALE_LOG2E, -m)); ps += s1[r]; }
      l += ps;
      bf16x8 pb[4];
#pragma unroll
      for (int ks = 0; ks < 2; ++ks) {
        u32x4 u = {pack2(s0[8 * ks], s0[8 * ks + 1]), pack2(s0[8 * ks + 2], s0[8 * ks + 3]), pack2(s0[8 * ks + 4], s0[8 * ks + 5]), pack2(s0[8 * ks + 6], s0[8 * ks + 7])};
        pb[ks] = __builtin_bit_cast(bf16x8, u);
        u32x4 v = {pack2(s1[8 * ks], s1[8 * ks + 1]), pack2(s1[8 * ks + 2], s1[8 * ks + 3]), pack2(s1[8 * ks + 4], s1[8 * ks + 5]), pack2(s1[8 * ks + 6], s1[8 * ks + 7])};
        pb[2 + ks] = __builtin_bit_cast(bf16x8, v);
      }
      const unsigned vb0 = sbase + vaddr0;
#pragma unroll
      for (int ks = 0; ks < 4; ++ks)
#pragma unroll
        for (int d = 0; d < 4; ++d) {
          const s16x4 lo = __builtin_amdgcn_ds_read_tr16_b64_v4i16((LDS_AS s16x4*)(sm + (vb0 ^ (d << 6)) + ks * 4096));
          const s16x4 up = __builtin_amdgcn_ds_read_tr16_b64_v4i16((LDS_AS s16x4*)(sm + (vb0 ^ ((d << 6) | 32)) + ks * 4096 + 2048));
          const bf16x8 a = __builtin_shufflevector(lo, up, 0, 1, 2, 3, 4, 5, 6, 7);
          o[d] = __builtin_amdgcn_mfma_f32_32x32x16_bf16(a, pb[ks], o[d], 0, 0, 0);
        }
    }
    flush();
    cc.t = cc.nt - 1; cc.isq = 0; cur_advance(cc, lens);
  }
}

__global__ __launch_bounds__(256) void attn_combine(const int* __restrict__ lens, const float* __restrict__ po, const float* __restrict__ pml,
                                                    __hip_bfloat16* __restrict__ out) {
  const int p = blockIdx.x >> 2, qb = blockIdx.x & 3, d0 = threadIdx.x >> 6, lane = threadIdx.x & 63, r32 = lane & 31, hi = lane >> 5;
  const int b = p >> 3, h = p & 7;
  int tot = 0, P = 0, ntb = 1;
#pragma unroll
  for (int bb = 0; bb < B; ++bb) { const int nt = (eff_len(lens[bb]) + TILE - 1) / TILE; if (bb == b) { P = tot * HKV + h * nt; ntb = nt; } tot += nt; }
  const int T = tot * HKV;
  const int i0 = ((P + 1) * NWG + T - 1) / T - 1, i1 = ((P + ntb) * NWG + T - 1) / T - 1;
  float M = -__builtin_inff();
  for (int i = i0; i <= i1; ++i) { if (i * T / NWG == (i + 1) * T / NWG) continue;
    M = fmaxf(M, pml[((size_t)(i + p) * 128 + qb * 32 + r32) * 2]); }
  float L = 0.f; f32x4 acc[4] = {};
  for (int i = i0; i <= i1; ++i) { if (i * T / NWG == (i + 1) * T / NWG) continue;
    const f32x2 ml = *(const f32x2*)(pml + ((size_t)(i + p) * 128 + qb * 32 + r32) * 2);
    const float f = __builtin_amdgcn_exp2f(ml[0] - M) * ml[1]; L += f;
    const unsigned* src = (const unsigned*)po + (size_t)(i + p) * (64 * D) + (size_t)(qb * 4 + d0) * 512 + lane * 2;
#pragma unroll
    for (int g = 0; g < 4; ++g) { const u32x2 x = *(const u32x2*)(src + g * 128);
      acc[g][0] += __uint_as_float(x[0] << 16) * f; acc[g][1] += __uint_as_float(x[0] & 0xffff0000u) * f;
      acc[g][2] += __uint_as_float(x[1] << 16) * f; acc[g][3] += __uint_as_float(x[1] & 0xffff0000u) * f; }
  }
  const float inv = 1.f / L;
  __hip_bfloat16* orow = out + ((size_t)((b * HQ + h * G) * SQ) + qb * 32 + r32) * D + 32 * d0 + 4 * hi;
#pragma unroll
  for (int g = 0; g < 4; ++g) { u32x2 u = {pack2(acc[g][0] * inv, acc[g][1] * inv), pack2(acc[g][2] * inv, acc[g][3] * inv)};
    *(u32x2*)(orow + 8 * g) = u; }
}

}

extern "C" void kernel_launch(void* const* d_in, const int* in_sizes, int n_in, void* d_out, int out_size, void* d_ws, size_t ws_size,
                              hipStream_t stream) {
  const __hip_bfloat16* q = (const __hip_bfloat16*)d_in[0];
  const __hip_bfloat16* kc = (const __hip_bfloat16*)d_in[1];
  const __hip_bfloat16* vc = (const __hip_bfloat16*)d_in[2];
  const int* bt = (const int*)d_in[3];
  const int* lens = (const int*)d_in[4];
  float* pml = (float*)d_ws;
  float* po = (float*)((char*)d_ws + (1u << 20));
  attn_split<<<NWG, 512, 0, stream>>>(q, kc, vc, bt, lens, po, pml);
  attn_combine<<<B * HKV * 4, 256, 0, stream>>>(lens, po, pml, (__hip_bfloat16*)d_out);
}
```
